```python
import torch

def _softmax(x, axis=-1):
    return torch.softmax(x, dim=axis)
def _gelu(x):
    return torch.nn.functional.gelu(x, approximate="tanh")
def _silu(x):
    return torch.nn.functional.silu(x)
def _sigmoid(x):
    return torch.sigmoid(x)
def _layernorm(x, axis=-1, eps=1e-5):
    m = x.mean(dim=axis, keepdim=True)
    v = x.var(dim=axis, keepdim=True, unbiased=False)
    return (x - m) / torch.sqrt(v + eps)
def _scatter_add(tgt, idx, src, axis=0):
    return tgt.clone().index_add_(axis, idx, src)
def _segment_sum(src, seg, n):
    out = torch.zeros((n,) + src.shape[1:], dtype=src.dtype)
    return out.index_add_(0, seg, src)
def _pad(x, pw):
    flat = tuple(v for pair in reversed(pw) for v in pair)
    return torch.nn.functional.pad(x, flat)

kernel_name = 'sequential_preLN__spectral_fnet__swiglu_ffn__prompt'

def setup_inputs():

    _w = (lambda a: (lambda t: t.to(torch.bfloat16) if t.is_floating_point() else t)(torch.as_tensor(a)))
    def _A_setup():
        B, S, D = 1, 2048, 512
        ns = torch.arange(S)[:, None] * torch.arange(S)[None, :]
        nd = torch.arange(D)[:, None] * torch.arange(D)[None, :]
        Ws_re = torch.cos(-2.0 * torch.pi * ns / S).to(float)
        Ws_im = torch.sin(-2.0 * torch.pi * ns / S).to(float)
        Wd_re = torch.cos(-2.0 * torch.pi * nd / D).to(float)
        Wd_im = torch.sin(-2.0 * torch.pi * nd / D).to(float)
        return {
            "x": _w((lambda *s: torch.randn(*s, dtype=torch.bfloat16) * 0.05)(B, S, D)),
            "dft_seq_re": _w(Ws_re),
            "dft_seq_im": _w(Ws_im),
            "dft_hid_re": _w(Wd_re),
            "dft_hid_im": _w(Wd_im),
        }
    _A_in = _A_setup()

    def _B_setup():
        B, S, D, F = 1, 2048, 512, 1376
        return {
            "x": _w((lambda *s: torch.randn(*s, dtype=torch.bfloat16) * 0.05)(B, S, D)),
            "w_gate": _w((lambda *s: torch.randn(*s, dtype=torch.bfloat16) * 0.2)(D, F)),
            "w_up": _w((lambda *s: torch.randn(*s, dtype=torch.bfloat16) * 0.2)(D, F)),
            "w_down": _w((lambda *s: torch.randn(*s, dtype=torch.bfloat16) * 0.2)(F, D)),
        }
    _B_in = _B_setup()

    return {
        "A_x": _A_in["x"],
        "A_dft_seq_re": _A_in["dft_seq_re"],
        "A_dft_seq_im": _A_in["dft_seq_im"],
        "A_dft_hid_re": _A_in["dft_hid_re"],
        "A_dft_hid_im": _A_in["dft_hid_im"],
        "B_x": _B_in["x"],
        "B_w_gate": _B_in["w_gate"],
        "B_w_up": _B_in["w_up"],
        "B_w_down": _B_in["w_down"],
    }

def reference(A_x, A_dft_seq_re, A_dft_seq_im, A_dft_hid_re, A_dft_hid_im, B_x, B_w_gate, B_w_up, B_w_down):
    def _A_ref(x, dft_seq_re, dft_seq_im, dft_hid_re, dft_hid_im):
        a = torch.einsum("bsd,ed->bse", x, dft_hid_re)
        b = torch.einsum("bsd,ed->bse", x, dft_hid_im)
        y = torch.einsum("ts,bse->bte", dft_seq_re, a) - torch.einsum("ts,bse->bte", dft_seq_im, b)
        return y

    def _B_ref(x, w_gate, w_up, w_down):
        g = _silu(x @ w_gate)
        u = x @ w_up
        return (g * u) @ w_down

    x = A_x
    h = x + _A_ref(_layernorm(x, axis=-1), A_dft_seq_re, A_dft_seq_im, A_dft_hid_re, A_dft_hid_im)
    h2 = h + _B_ref(_layernorm(h, axis=-1), B_w_gate, B_w_up, B_w_down)
    return h2
```

```cpp
#include <hip/hip_runtime.h>
#include <hip/hip_bf16.h>
typedef unsigned short u16;
using bf16x8 = __attribute__((ext_vector_type(8))) short;
using f32x4 = __attribute__((ext_vector_type(4))) float;
constexpr int S=2048, D=512, F=1376;
__device__ __forceinline__ float bf2f(u16 v){ return __uint_as_float(((unsigned)v)<<16); }
__device__ __forceinline__ u16 f2bf(float f){ __hip_bfloat16 h = __float2bfloat16(f); return *(u16*)&h; }

__global__ void ln_kernel(const u16* in, u16* out){
  int row = blockIdx.x*4 + (threadIdx.x>>6); int lane = threadIdx.x&63;
  float v[8]; float s=0;
  for(int j=0;j<8;++j){ v[j]=bf2f(in[row*D+lane*8+j]); s+=v[j]; }
  for(int o=32;o;o>>=1) s+=__shfl_xor(s,o);
  float m=s/D; float q=0;
  for(int j=0;j<8;++j){ float d=v[j]-m; q+=d*d; }
  for(int o=32;o;o>>=1) q+=__shfl_xor(q,o);
  float r=rsqrtf(q/D+1e-5f);
  for(int j=0;j<8;++j) out[row*D+lane*8+j]=f2bf((v[j]-m)*r);
}
template<int MODE>
__global__ void gemm_kernel(const u16* A, int lda, const u16* B, int sk, int sn, int K,
                            const u16* A2, const u16* B2, const u16* res, u16* C, int ldc, int N){
  int wave = blockIdx.x*4 + (threadIdx.x>>6); int lane=threadIdx.x&63;
  int nt = N/16; int tm = wave/nt, tn = wave%nt;
  int r=lane&15, q=lane>>4;
  f32x4 acc={0,0,0,0}, acc2={0,0,0,0};
  for(int k0=0;k0<K;k0+=32){
    bf16x8 a = *(const bf16x8*)(A+(long)(tm*16+r)*lda+k0+q*8);
    bf16x8 b; for(int j=0;j<8;++j) b[j]=B[(long)(k0+q*8+j)*sk+(long)(tn*16+r)*sn];
    acc=__builtin_amdgcn_mfma_f32_16x16x32_bf16(a,b,acc,0,0,0);
    if(MODE==1||MODE==2){
      bf16x8 a2 = *(const bf16x8*)(A2+(long)(tm*16+r)*lda+k0+q*8);
      bf16x8 b2; for(int j=0;j<8;++j) b2[j]=B2[(long)(k0+q*8+j)*sk+(long)(tn*16+r)*sn];
      acc2=__builtin_amdgcn_mfma_f32_16x16x32_bf16(a2,b2,acc2,0,0,0);
    }
  }
  for(int j=0;j<4;++j){
    int row=tm*16+q*4+j, col=tn*16+r; float v=acc[j];
    if(MODE==1) v = acc[j]-acc2[j]+bf2f(res[row*ldc+col]);
    if(MODE==2) v = acc[j]/(1.f+__expf(-acc[j]))*acc2[j];
    if(MODE==3) v = acc[j]+bf2f(res[row*ldc+col]);
    C[(long)row*ldc+col]=f2bf(v);
  }
}
extern "C" void kernel_launch(void* const* d_in, const int* in_sizes, int n_in,
                              void* d_out, int out_size, void* d_ws, size_t ws_size,
                              hipStream_t stream) {
  const u16* x=(const u16*)d_in[0]; const u16* sre=(const u16*)d_in[1]; const u16* sim=(const u16*)d_in[2];
  const u16* hre=(const u16*)d_in[3]; const u16* him=(const u16*)d_in[4];
  const u16* wg=(const u16*)d_in[6]; const u16* wu=(const u16*)d_in[7]; const u16* wd=(const u16*)d_in[8];
  u16* ws=(u16*)d_ws; u16* xn=ws; u16* a=xn+S*D; u16* b=a+S*D; u16* h=b+S*D; u16* hn=h+S*D; u16* act=hn+S*D;
  u16* out=(u16*)d_out;
  ln_kernel<<<S/4,256,0,stream>>>(x,xn);
  gemm_kernel<0><<<S/16*D/16/4,256,0,stream>>>(xn,D,hre,1,D,D,nullptr,nullptr,nullptr,a,D,D);
  gemm_kernel<0><<<S/16*D/16/4,256,0,stream>>>(xn,D,him,1,D,D,nullptr,nullptr,nullptr,b,D,D);
  gemm_kernel<1><<<S/16*D/16/4,256,0,stream>>>(sre,S,a,D,1,S,sim,b,x,h,D,D);
  ln_kernel<<<S/4,256,0,stream>>>(h,hn);
  gemm_kernel<2><<<S/16*F/16/4,256,0,stream>>>(hn,D,wg,F,1,D,hn,wu,nullptr,act,F,F);
  gemm_kernel<3><<<S/16*D/16/4,256,0,stream>>>(act,F,wd,D,1,F,nullptr,nullptr,h,out,D,D);
}
```

```cpp
#include <hip/hip_runtime.h>

__global__ __launch_bounds__(256, 1) void fnet_swiglu_kernel(const void* x, const void* sre, const void* sim,
                                                             const void* hre, const void* him, const void* wg,
                                                             const void* wu, const void* wd, void* out, void* ws) {
  __shared__ __attribute__((aligned(16))) char lds[163840];
  unsigned tid = threadIdx.x;
  asm volatile(
      "s_load_dwordx16 s[40:55], %1, 0\n"
      "s_load_dwordx4 s[56:59], %1, 64\n"
      "s_getpc_b64 s[36:37]\n"
      "Lpc_%=:\n"
      "v_readfirstlane_b32 s63, v0\n"
      "s_mov_b32 s62, %2\n"
      "v_and_b32 v0, 63, v0\n"
      "s_and_b32 s37, s37, 0xffff\n"
      "s_mov_b32 s38, Lend_%=-Lpc_%=\n"
      "s_mov_b32 s39, 0x20000\n"
      "s_lshr_b32 s63, s63, 6\n"
      "s_lshr_b32 s70, s62, 3\n"
      "s_lshl2_add_u32 s70, s70, s63\n"
      "s_lshl_b32 s70, s70, 7\n"
      "v_lshl_add_u32 v250, v0, 14, s70\n"
      "buffer_load_dword v251, v250, s[36:39], 0 offen\n"
      "s_and_b32 s64, s62, 3\n"
      "s_bfe_u32 s67, s62, 0x10002\n"
      "s_bfe_u32 s65, s62, 0x20003\n"
      "s_lshr_b32 s66, s62, 5\n"
      "s_lshl_b32 s69, s63, 11\n"
      "v_lshrrev_b32 v249, 4, v0\n"
      "v_and_b32 v6, 15, v0\n"
      "v_lshl_add_u32 v4, s63, 3, v249\n"
      "v_lshl_add_u32 v5, s63, 2, v249\n"
      "v_xor_b32 v5, v5, v6\n"
      "v_lshlrev_b32 v5, 4, v5\n"
      "v_lshl_add_u32 v2, v4, 10, v5\n"
      "v_add_u32 v3, 0xc00, v2\n"
      "s_lshl3_add_u32 s83, s64, s66\n"
      "s_add_u32 s70, s66, s65\n"
      "s_and_b32 s70, s70, 3\n"
      "s_lshl_b32 s70, s70, 8\n"
      "s_lshl_b32 s71, s83, 16\n"
      "s_lshl_b32 s72, s65, 17\n"
      "s_add_u32 s75, s71, s70\n"
      "s_add_u32 s79, s72, s70\n"
      "s_mov_b32 s6, 0x200000\n"
      "s_mov_b32 s7, s39\n"
      "s_mov_b32 s10, 0x80000\n"
      "s_mov_b32 s11, s39\n"
      "s_cmp_eq_u32 s67, 0\n"
      "s_waitcnt lgkmcnt(0)\n"
      "s_cselect_b64 s[8:9], s[46:47], s[48:49]\n"
      "s_mov_b32 s4, s40\n"
      "s_and_b32 s5, s41, 0xffff\n"
      "s_and_b32 s9, s9, 0xffff\n"
      "s_add_u32 m0, s69, 0\n"
      "s_nop 0\n"
      "buffer_load_dwordx4 v2, s[4:7], s75 offen lds\n"
      "buffer_load_dwordx4 v3, s[4:7], s75 offen offset:1024 lds\n"
      "s_add_u32 m0, s69, 0x2000\n"
      "s_add_u32 s74, s75, 0x8000\n"
      "buffer_load_dwordx4 v2, s[4:7], s74 offen lds\n"
      "buffer_load_dwordx4 v3, s[4:7], s74 offen offset:1024 lds\n"
      "s_add_u32 m0, s69, 0x4000\n"
      "s_nop 0\n"
      "buffer_load_dwordx4 v2, s[8:11], s79 offen lds\n"
      "buffer_load_dwordx4 v3, s[8:11], s79 offen offset:1024 lds\n"
      "s_add_u32 m0, s69, 0x6000\n"
      "s_add_u32 s74, s79, 0x8000\n"
      "buffer_load_dwordx4 v2, s[8:11], s74 offen lds\n"
      "buffer_load_dwordx4 v3, s[8:11], s74 offen offset:1024 lds\n"
      "s_load_dwordx2 s[60:61], s[58:59], 0\n"
      "s_add_u32 s70, s70, 0x100\n"
      "s_and_b32 s70, s70, 0x300\n"
      "s_add_u32 s76, s71, s70\n"
      "s_add_u32 s80, s72, s70\n"
      "s_add_u32 s70, s70, 0x100\n"
      "s_and_b32 s70, s70, 0x300\n"
      "s_add_u32 s77, s71, s70\n"
      "s_add_u32 s81, s72, s70\n"
      "s_add_u32 s70, s70, 0x100\n"
      "s_and_b32 s70, s70, 0x300\n"
      "s_add_u32 s78, s71, s70\n"
      "s_add_u32 s82, s72, s70\n"
      "v_and_b32 v254, 12, v0\n"
      "v_add_u32 v7, v254, v6\n"
      "v_xor_b32 v254, v249, v6\n"
      "v_lshlrev_b32 v254, 4, v254\n"
      "v_lshl_add_u32 v8, v7, 8, v254\n"
      "s_lshr_b32 s70, s63, 1\n"
      "s_lshl_b32 s70, s70, 6\n"
      "v_xor_b32 v254, s70, v8\n"
      "s_and_b32 s70, s63, 1\n"
      "s_lshl_b32 s70, s70, 13\n"
      "s_xor_b32 s71, s70, 0x2000\n"
      "s_lshl_b32 s72, s63, 13\n"
      "s_add_u32 s72, s72, 0x4000\n"
      "v_add_u32 v9, s70, v254\n"
      "v_add_u32 v17, s71, v254\n"
      "v_add_u32 v25, s72, v254\n"
      "v_xor_b32 v10, 64, v9\n"
      "v_xor_b32 v18, 64, v17\n"
      "v_xor_b32 v26, 64, v25\n"
      "v_xor_b32 v11, 0x80, v9\n"
      "v_xor_b32 v19, 0x80, v17\n"
      "v_xor_b32 v27, 0x80, v25\n"
      "v_xor_b32 v12, 0xc0, v9\n"
      "v_xor_b32 v20, 0xc0, v17\n"
      "v_xor_b32 v28, 0xc0, v25\n"
      "v_add_u32 v13, 0x18000, v9\n"
      "v_add_u32 v21, 0x18000, v17\n"
      "v_add_u32 v29, 0x18000, v25\n"
      "v_add_u32 v14, 0x18000, v10\n"
      "v_add_u32 v22, 0x18000, v18\n"
      "v_add_u32 v30, 0x18000, v26\n"
      "v_add_u32 v15, 0x18000, v11\n"
      "v_add_u32 v23, 0x18000, v19\n"
      "v_add_u32 v31, 0x18000, v27\n"
      "v_add_u32 v16, 0x18000, v12\n"
      "v_add_u32 v24, 0x18000, v20\n"
      "v_add_u32 v32, 0x18000, v28\n"
      "v_mov_b32 v248, 0x3f803f80\n"
      "v_mov_b32 v34, 0\n"
      "v_mov_b32 v36, 0\n"
      "v_mov_b32 v38, 0\n"
      "v_mov_b32 v39, 0\n"
      "v_mov_b32 v40, 0\n"
      "v_mov_b32 v41, 0\n"
      "s_mov_b32 s16, s50\n"
      "s_and_b32 s17, s51, 0xffff\n"
      "s_mov_b32 s18, 0x158000\n"
      "s_mov_b32 s19, s39\n"
      "s_mov_b32 s20, s52\n"
      "s_and_b32 s21, s53, 0xffff\n"
      "s_mov_b32 s22, 0x158000\n"
      "s_mov_b32 s23, s39\n"
      "s_mov_b32 s24, s54\n"
      "s_and_b32 s25, s55, 0xffff\n"
      "s_mov_b32 s26, 0x158000\n"
      "s_mov_b32 s27, s39\n"
      "s_cmp_eq_u32 s63, 0\n"
      "s_cselect_b64 s[48:49], s[16:17], s[20:21]\n"
      "s_cmp_eq_u32 s63, 2\n"
      "s_cselect_b64 s[48:49], s[24:25], s[48:49]\n"
      "s_mov_b64 s[50:51], s[18:19]\n"
      "s_cmp_eq_u32 s63, 3\n"
      "s_cselect_b32 s70, 0, 43\n"
      "s_mul_i32 s71, s62, 0x1580\n"
      "v_lshl_add_u32 v254, v0, 7, s71\n"
      "v_mov_b32 v253, 0x40000000\n"
      "v_cmp_gt_u32 vcc, s70, v0\n"
      "s_nop 1\n"
      "v_cndmask_b32 v33, v253, v254, vcc\n"
      "s_bitcmp0_b32 s64, 1\n"
      "s_cselect_b64 s[12:13], s[42:43], s[44:45]\n"
      "s_and_b32 s13, s13, 0xffff\n"
      "s_mov_b32 s14, 0x800000\n"
      "s_mov_b32 s15, s39\n"
      "s_lshl3_add_u32 s68, s67, s66\n"
      "v_lshl_add_u32 v4, v4, 12, v5\n"
      "v_add_u32 v5, 0x3c00, v4\n"
      "s_add_u32 s40, s64, s65\n"
      "s_and_b32 s40, s40, 3\n"
      "s_and_b32 s71, s64, 1\n"
      "s_lshl_b32 s71, s71, 11\n"
      "s_add_u32 s70, s40, 1\n"
      "s_and_b32 s70, s70, 3\n"
      "s_lshl2_add_u32 s70, s68, s70\n"
      "s_lshl_b32 s70, s70, 17\n"
      "s_add_u32 s41, s70, s71\n"
      "s_add_u32 s70, s40, 2\n"
      "s_and_b32 s70, s70, 3\n"
      "s_lshl2_add_u32 s70, s68, s70\n"
      "s_lshl_b32 s70, s70, 17\n"
      "s_add_u32 s42, s70, s71\n"
      "s_add_u32 s70, s40, 3\n"
      "s_and_b32 s70, s70, 3\n"
      "s_lshl2_add_u32 s70, s68, s70\n"
      "s_lshl_b32 s70, s70, 17\n"
      "s_add_u32 s43, s70, s71\n"
      "s_add_u32 s70, s40, 4\n"
      "s_and_b32 s70, s70, 3\n"
      "s_lshl2_add_u32 s70, s68, s70\n"
      "s_lshl_b32 s70, s70, 17\n"
      "s_add_u32 s44, s70, s71\n"
      "s_mov_b32 s32, s58\n"
      "s_and_b32 s33, s59, 0xffff\n"
      "s_mov_b32 s34, 0x3800000\n"
      "s_mov_b32 s35, s39\n"
      "s_sub_u32 s45, 0, s58\n"
      "s_and_b32 s45, s45, 0x1fffff\n"
      "s_lshl_b32 s70, s68, 21\n"
      "s_add_u32 s45, s45, s70\n"
      "s_add_u32 s45, s45, 0x1600000\n"
      "s_lshl2_add_u32 s70, s64, s63\n"
      "s_mul_i32 s70, s70, 0x2080\n"
      "s_lshl_b32 s71, s65, 11\n"
      "s_add_u32 s70, s70, s71\n"
      "s_add_u32 s70, s70, s45\n"
      "s_add_u32 s46, s70, 0x1e0000\n"
      "v_lshlrev_b32 v254, 4, v0\n"
      "v_cmp_gt_u32 vcc, 8, v0\n"
      "s_nop 1\n"
      "v_cndmask_b32 v254, v253, v254, vcc\n"
      "buffer_store_dwordx4 v[34:37], v254, s[32:35], s46 offen sc1\n"
      "s_add_u32 m0, s69, 0x8000\n"
      "s_add_u32 s74, s79, 0x10000\n"
      "buffer_load_dwordx4 v2, s[8:11], s74 offen lds\n"
      "buffer_load_dwordx4 v3, s[8:11], s74 offen offset:1024 lds\n"
      "s_add_u32 m0, s69, 0xa000\n"
      "s_add_u32 s74, s79, 0x18000\n"
      "buffer_load_dwordx4 v2, s[8:11], s74 offen lds\n"
      "buffer_load_dwordx4 v3, s[8:11], s74 offen offset:1024 lds\n"
      "s_add_u32 m0, s69, 0xc000\n"
      "s_nop 0\n"
      "buffer_load_dwordx4 v2, s[4:7], s76 offen lds\n"
      "buffer_load_dwordx4 v3, s[4:7], s76 offen offset:1024 lds\n"
      "s_add_u32 m0, s69, 0xe000\n"
      "s_add_u32 s74, s76, 0x8000\n"
      "buffer_load_dwordx4 v2, s[4:7], s74 offen lds\n"
      "buffer_load_dwordx4 v3, s[4:7], s74 offen offset:1024 lds\n"
      "s_add_u32 m0, s69, 0x10000\n"
      "s_nop 0\n"
      "buffer_load_dwordx4 v2, s[8:11], s80 offen lds\n"
      "buffer_load_dwordx4 v3, s[8:11], s80 offen offset:1024 lds\n"
      "s_add_u32 m0, s69, 0x12000\n"
      "s_add_u32 s74, s80, 0x8000\n"
      "buffer_load_dwordx4 v2, s[8:11], s74 offen lds\n"
      "buffer_load_dwordx4 v3, s[8:11], s74 offen offset:1024 lds\n"
      "s_add_u32 m0, s69, 0x14000\n"
      "s_add_u32 s74, s80, 0x10000\n"
      "buffer_load_dwordx4 v2, s[8:11], s74 offen lds\n"
      "buffer_load_dwordx4 v3, s[8:11], s74 offen offset:1024 lds\n"
      "s_add_u32 m0, s69, 0x16000\n"
      "s_add_u32 s74, s80, 0x18000\n"
      "buffer_load_dwordx4 v2, s[8:11], s74 offen lds\n"
      "buffer_load_dwordx4 v3, s[8:11], s74 offen offset:1024 lds\n"
      "s_add_u32 m0, s69, 0x18000\n"
      "s_nop 0\n"
      "buffer_load_dwordx4 v2, s[4:7], s77 offen lds\n"
      "s_waitcnt vmcnt(13) lgkmcnt(0)\n"
      "s_barrier\n"
      "ds_read_b128 v[52:55], v9\n"
      "ds_read_b128 v[56:59], v9 offset:1024\n"
      "ds_read_b128 v[60:63], v17\n"
      "ds_read_b128 v[64:67], v17 offset:1024\n"
      "ds_read_b128 v[68:71], v25\n"
      "ds_read_b128 v[72:75], v25 offset:1024\n"
      "s_waitcnt lgkmcnt(0)\n"
      "v_mfma_f32_16x16x32_bf16 v[100:103], v[52:55], v[68:71], 0\n"
      "s_lshl_b32 s70, s63, 8\n"
      "s_add_u32 s70, s70, 0x26000\n"
      "v_lshl_add_u32 v42, v0, 2, s70\n"
      "v_mfma_f32_16x16x32_bf16 v[108:111], v[52:55], v[72:75], 0\n"
      "ds_read_b128 v[76:79], v10\n"
      "ds_read_b128 v[80:83], v10 offset:1024\n"
      "v_mfma_f32_16x16x32_bf16 v[104:107], v[56:59], v[68:71], 0\n"
      "buffer_load_dwordx4 v3, s[4:7], s77 offen offset:1024 lds\n"
      "v_mfma_f32_16x16x32_bf16 v[112:115], v[56:59], v[72:75], 0\n"
      "s_add_u32 m0, s69, 0x1a000\n"
      "ds_read_b128 v[84:87], v18\n"
      "ds_read_b128 v[88:91], v18 offset:1024\n"
      "v_mfma_f32_16x16x32_bf16 v[116:119], v[60:63], v[68:71], 0\n"
      "s_add_u32 s74, s77, 0x8000\n"
      "buffer_load_dwordx4 v2, s[4:7], s74 offen lds\n"
      "v_mfma_f32_16x16x32_bf16 v[124:127], v[60:63], v[72:75], 0\n"
      "ds_read_b128 v[92:95], v26\n"
      "ds_read_b128 v[96:99], v26 offset:1024\n"
      "v_mfma_f32_16x16x32_bf16 v[120:123], v[64:67], v[68:71], 0\n"
      "v_lshlrev_b32 v254, 7, v249\n"
      "v_lshl_add_u32 v254, v6, 2, v254\n"
      "v_mfma_f32_16x16x32_bf16 v[128:131], v[64:67], v[72:75], 0\n"
      "buffer_load_dwordx4 v3, s[4:7], s74 offen offset:1024 lds\n"
      "v_add_u32 v43, 0x26000, v254\n"
      "s_lshl_b32 s70, s63, 9\n"
      "s_add_u32 s70, s70, 0x26400\n"
      "v_dot2c_f32_bf16 v38, v52, v248\n"
      "v_dot2c_f32_bf16 v39, v52, v52\n"
      "v_dot2c_f32_bf16 v40, v56, v248\n"
      "v_dot2c_f32_bf16 v41, v56, v56\n"
      "v_dot2c_f32_bf16 v38, v53, v248\n"
      "v_dot2c_f32_bf16 v39, v53, v53\n"
      "v_dot2c_f32_bf16 v40, v57, v248\n"
      "v_dot2c_f32_bf16 v41, v57, v57\n"
      "v_dot2c_f32_bf16 v38, v54, v248\n"
      "v_dot2c_f32_bf16 v39, v54, v54\n"
      "v_dot2c_f32_bf16 v40, v58, v248\n"
      "v_dot2c_f32_bf16 v41, v58, v58\n"
      "v_dot2c_f32_bf16 v38, v55, v248\n"
      "v_dot2c_f32_bf16 v39, v55, v55\n"
      "v_dot2c_f32_bf16 v40, v59, v248\n"
      "v_dot2c_f32_bf16 v41, v59, v59\n"
      "v_dot2c_f32_bf16 v34, v68, v248\n"
      "v_dot2c_f32_bf16 v36, v72, v248\n"
      "v_dot2c_f32_bf16 v34, v69, v248\n"
      "v_dot2c_f32_bf16 v36, v73, v248\n"
      "v_dot2c_f32_bf16 v34, v70, v248\n"
      "v_dot2c_f32_bf16 v36, v74, v248\n"
      "v_dot2c_f32_bf16 v34, v71, v248\n"
      "v_dot2c_f32_bf16 v36, v75, v248\n"
      "s_waitcnt lgkmcnt(0)\n"
      "v_mfma_f32_16x16x32_bf16 v[100:103], v[76:79], v[92:95], v[100:103]\n"
      "s_add_u32 m0, s69, 0x1c000\n"
      "v_lshl_add_u32 v44, v0, 2, s70\n"
      "s_and_b32 s71, s63, 1\n"
      "v_mfma_f32_16x16x32_bf16 v[108:111], v[76:79], v[96:99], v[108:111]\n"
      "ds_read_b128 v[52:55], v11\n"
      "ds_read_b128 v[56:59], v11 offset:1024\n"
      "v_mfma_f32_16x16x32_bf16 v[104:107], v[80:83], v[92:95], v[104:107]\n"
      "buffer_load_dwordx4 v2, s[8:11], s81 offen lds\n"
      "v_mfma_f32_16x16x32_bf16 v[112:115], v[80:83], v[96:99], v[112:115]\n"
      "ds_read_b128 v[60:63], v19\n"
      "ds_read_b128 v[64:67], v19 offset:1024\n"
      "v_mfma_f32_16x16x32_bf16 v[116:119], v[84:87], v[92:95], v[116:119]\n"
      "buffer_load_dwordx4 v3, s[8:11], s81 offen offset:1024 lds\n"
      "v_mfma_f32_16x16x32_bf16 v[124:127], v[84:87], v[96:99], v[124:127]\n"
      "s_add_u32 m0, s69, 0x1e000\n"
      "ds_read_b128 v[68:71], v27\n"
      "ds_read_b128 v[72:75], v27 offset:1024\n"
      "v_mfma_f32_16x16x32_bf16 v[120:123], v[88:91], v[92:95], v[120:123]\n"
      "s_add_u32 s74, s81, 0x8000\n"
      "s_lshl_b32 s71, s71, 7\n"
      "s_add_u32 s70, s70, s71\n"
      "v_mfma_f32_16x16x32_bf16 v[128:131], v[88:91], v[96:99], v[128:131]\n"
      "buffer_load_dwordx4 v2, s[8:11], s74 offen lds\n"
      "v_lshl_add_u32 v45, v249, 4, s70\n"
      "v_xor_b32 v46, 0x80, v45\n"
      "s_lshl2_add_u32 s70, s67, s65\n"
      "v_dot2c_f32_bf16 v34, v92, v248\n"
      "v_dot2c_f32_bf16 v36, v96, v248\n"
      "v_dot2c_f32_bf16 v34, v93, v248\n"
      "v_dot2c_f32_bf16 v36, v97, v248\n"
      "v_dot2c_f32_bf16 v34, v94, v248\n"
      "v_dot2c_f32_bf16 v36, v98, v248\n"
      "v_dot2c_f32_bf16 v34, v95, v248\n"
      "v_dot2c_f32_bf16 v36, v99, v248\n"
      "s_waitcnt lgkmcnt(0)\n"
      "v_mfma_f32_16x16x32_bf16 v[100:103], v[52:55], v[68:71], v[100:103]\n"
      "s_lshl2_add_u32 s70, s70, s63\n"
      "s_lshl_b32 s70, s70, 17\n"
      "s_lshl_b32 s71, s83, 12\n"
      "v_mfma_f32_16x16x32_bf16 v[108:111], v[52:55], v[72:75], v[108:111]\n"
      "ds_read_b128 v[76:79], v12\n"
      "ds_read_b128 v[80:83], v12 offset:1024\n"
      "v_mfma_f32_16x16x32_bf16 v[104:107], v[56:59], v[68:71], v[104:107]\n"
      "buffer_load_dwordx4 v3, s[8:11], s74 offen offset:1024 lds\n"
      "v_mfma_f32_16x16x32_bf16 v[112:115], v[56:59], v[72:75], v[112:115]\n"
      "ds_read_b128 v[84:87], v20\n"
      "ds_read_b128 v[88:91], v20 offset:1024\n"
      "v_mfma_f32_16x16x32_bf16 v[116:119], v[60:63], v[68:71], v[116:119]\n"
      "buffer_load_dword v251, v33, s[48:51], 0 offen\n"
      "v_mfma_f32_16x16x32_bf16 v[124:127], v[60:63], v[72:75], v[124:127]\n"
      "s_add_u32 m0, s69, 0x20000\n"
      "ds_read_b128 v[92:95], v28\n"
      "ds_read_b128 v[96:99], v28 offset:1024\n"
      "v_mfma_f32_16x16x32_bf16 v[120:123], v[64:67], v[68:71], v[120:123]\n"
      "s_add_u32 s74, s81, 0x10000\n"
      "s_add_u32 s70, s70, s71\n"
      "s_and_b32 s71, s63, 1\n"
      "v_mfma_f32_16x16x32_bf16 v[128:131], v[64:67], v[72:75], v[128:131]\n"
      "buffer_load_dwordx4 v2, s[8:11], s74 offen lds\n"
      "s_lshl_b32 s71, s71, 11\n"
      "s_add_u32 s70, s70, s71\n"
      "s_add_u32 s47, s70, 0x100000\n"
      "v_dot2c_f32_bf16 v38, v52, v248\n"
      "v_dot2c_f32_bf16 v39, v52, v52\n"
      "v_dot2c_f32_bf16 v40, v56, v248\n"
      "v_dot2c_f32_bf16 v41, v56, v56\n"
      "v_dot2c_f32_bf16 v38, v53, v248\n"
      "v_dot2c_f32_bf16 v39, v53, v53\n"
      "v_dot2c_f32_bf16 v40, v57, v248\n"
      "v_dot2c_f32_bf16 v41, v57, v57\n"
      "v_dot2c_f32_bf16 v38, v54, v248\n"
      "v_dot2c_f32_bf16 v39, v54, v54\n"
      "v_dot2c_f32_bf16 v40, v58, v248\n"
      "v_dot2c_f32_bf16 v41, v58, v58\n"
      "v_dot2c_f32_bf16 v38, v55, v248\n"
      "v_dot2c_f32_bf16 v39, v55, v55\n"
      "v_dot2c_f32_bf16 v40, v59, v248\n"
      "v_dot2c_f32_bf16 v41, v59, v59\n"
      "v_dot2c_f32_bf16 v34, v68, v248\n"
      "v_dot2c_f32_bf16 v36, v72, v248\n"
      "v_dot2c_f32_bf16 v34, v69, v248\n"
      "v_dot2c_f32_bf16 v36, v73, v248\n"
      "v_dot2c_f32_bf16 v34, v70, v248\n"
      "v_dot2c_f32_bf16 v36, v74, v248\n"
      "v_dot2c_f32_bf16 v34, v71, v248\n"
      "v_dot2c_f32_bf16 v36, v75, v248\n"
      "s_waitcnt lgkmcnt(0)\n"
      "v_mfma_f32_16x16x32_bf16 v[100:103], v[76:79], v[92:95], v[100:103]\n"
      "s_waitcnt vmcnt(10)\n"
      "s_barrier\n"
      "s_xor_b32 s52, s47, 0x800\n"
      "v_mfma_f32_16x16x32_bf16 v[108:111], v[76:79], v[96:99], v[108:111]\n"
      "ds_read_b128 v[52:55], v9 offset:49152\n"
      "ds_read_b128 v[56:59], v9 offset:50176\n"
      "v_mfma_f32_16x16x32_bf16 v[104:107], v[80:83], v[92:95], v[104:107]\n"
      "buffer_load_dwordx4 v3, s[8:11], s74 offen offset:1024 lds\n"
      "v_mfma_f32_16x16x32_bf16 v[112:115], v[80:83], v[96:99], v[112:115]\n"
      "s_add_u32 m0, s69, 0x22000\n"
      "ds_read_b128 v[60:63], v17 offset:49152\n"
      "ds_read_b128 v[64:67], v17 offset:50176\n"
      "v_mfma_f32_16x16x32_bf16 v[116:119], v[84:87], v[92:95], v[116:119]\n"
      "s_add_u32 s74, s81, 0x18000\n"
      "buffer_load_dwordx4 v2, s[8:11], s74 offen lds\n"
      "v_mfma_f32_16x16x32_bf16 v[124:127], v[84:87], v[96:99], v[124:127]\n"
      "ds_read_b128 v[68:71], v25 offset:49152\n"
      "ds_read_b128 v[72:75], v25 offset:50176\n"
      "v_mfma_f32_16x16x32_bf16 v[120:123], v[88:91], v[92:95], v[120:123]\n"
      "s_lshl_b32 s53, s67, 31\n"
      "s_mov_b32 s28, s56\n"
      "s_and_b32 s29, s57, 0xffff\n"
      "v_mfma_f32_16x16x32_bf16 v[128:131], v[88:91], v[96:99], v[128:131]\n"
      "buffer_load_dwordx4 v3, s[8:11], s74 offen offset:1024 lds\n"
      "s_mov_b32 s30, 0x200000\n"
      "s_mov_b32 s31, s39\n"
      "v_lshlrev_b32 v255, 4, v0\n"
      "v_dot2c_f32_bf16 v34, v92, v248\n"
      "v_dot2c_f32_bf16 v36, v96, v248\n"
      "v_dot2c_f32_bf16 v34, v93, v248\n"
      "v_dot2c_f32_bf16 v36, v97, v248\n"
      "v_dot2c_f32_bf16 v34, v94, v248\n"
      "v_dot2c_f32_bf16 v36, v98, v248\n"
      "v_dot2c_f32_bf16 v34, v95, v248\n"
      "v_dot2c_f32_bf16 v36, v99, v248\n"
      "s_waitcnt lgkmcnt(0)\n"
      "v_mfma_f32_16x16x32_bf16 v[100:103], v[52:55], v[68:71], v[100:103]\n"
      "s_add_u32 m0, s69, 0\n"
      "s_add_u32 s60, s60, 1\n"
      "s_addc_u32 s61, s61, 0\n"
      "v_mfma_f32_16x16x32_bf16 v[108:111], v[52:55], v[72:75], v[108:111]\n"
      "ds_read_b128 v[76:79], v10 offset:49152\n"
      "ds_read_b128 v[80:83], v10 offset:50176\n"
      "v_mfma_f32_16x16x32_bf16 v[104:107], v[56:59], v[68:71], v[104:107]\n"
      "buffer_load_dwordx4 v2, s[4:7], s78 offen lds\n"
      "v_mfma_f32_16x16x32_bf16 v[112:115], v[56:59], v[72:75], v[112:115]\n"
      "ds_read_b128 v[84:87], v18 offset:49152\n"
      "ds_read_b128 v[88:91], v18 offset:50176\n"
      "v_mfma_f32_16x16x32_bf16 v[116:119], v[60:63], v[68:71], v[116:119]\n"
      "buffer_load_dwordx4 v3, s[4:7], s78 offen offset:1024 lds\n"
      "v_mfma_f32_16x16x32_bf16 v[124:127], v[60:63], v[72:75], v[124:127]\n"
      "s_add_u32 m0, s69, 0x2000\n"
      "ds_read_b128 v[92:95], v26 offset:49152\n"
      "ds_read_b128 v[96:99], v26 offset:50176\n"
      "v_mfma_f32_16x16x32_bf16 v[120:123], v[64:67], v[68:71], v[120:123]\n"
      "s_add_u32 s74, s78, 0x8000\n"
      "v_mov_b32 v246, s60\n"
      "v_mov_b32 v247, s61\n"
      "v_mfma_f32_16x16x32_bf16 v[128:131], v[64:67], v[72:75], v[128:131]\n"
      "buffer_load_dwordx4 v2, s[4:7], s74 offen lds\n"
      "v_lshlrev_b32 v245, 3, v0\n"
      "v_lshl_add_u32 v254, s63, 6, v0\n"
      "v_mov_b32 v253, 0x40000000\n"
      "v_dot2c_f32_bf16 v38, v52, v248\n"
      "v_dot2c_f32_bf16 v39, v52, v52\n"
      "v_dot2c_f32_bf16 v40, v56, v248\n"
      "v_dot2c_f32_bf16 v41, v56, v56\n"
      "v_dot2c_f32_bf16 v38, v53, v248\n"
      "v_dot2c_f32_bf16 v39, v53, v53\n"
      "v_dot2c_f32_bf16 v40, v57, v248\n"
      "v_dot2c_f32_bf16 v41, v57, v57\n"
      "v_dot2c_f32_bf16 v38, v54, v248\n"
      "v_dot2c_f32_bf16 v39, v54, v54\n"
      "v_dot2c_f32_bf16 v40, v58, v248\n"
      "v_dot2c_f32_bf16 v41, v58, v58\n"
      "v_dot2c_f32_bf16 v38, v55, v248\n"
      "v_dot2c_f32_bf16 v39, v55, v55\n"
      "v_dot2c_f32_bf16 v40, v59, v248\n"
      "v_dot2c_f32_bf16 v41, v59, v59\n"
      "v_dot2c_f32_bf16 v34, v68, v248\n"
      "v_dot2c_f32_bf16 v36, v72, v248\n"
      "v_dot2c_f32_bf16 v34, v69, v248\n"
      "v_dot2c_f32_bf16 v36, v73, v248\n"
      "v_dot2c_f32_bf16 v34, v70, v248\n"
      "v_dot2c_f32_bf16 v36, v74, v248\n"
      "v_dot2c_f32_bf16 v34, v71, v248\n"
      "v_dot2c_f32_bf16 v36, v75, v248\n"
      "s_memtime s[54:55]\n"
      "s_waitcnt lgkmcnt(0)\n"
      "s_buffer_load_dword s73, s[32:35], s46\n"
      "s_lshl2_add_u32 s46, s63, 0x26c00\n"
      "v_mov_b32 v49, s46\n"
      "s_waitcnt lgkmcnt(0)\n"
      "s_memtime s[84:85]\n"
      "s_waitcnt lgkmcnt(0)\n"
      "s_sub_u32 s73, s84, s54\n"
      "v_mov_b32 v48, s73\n"
      "ds_write_b32 v49, v48\n"
      "s_waitcnt lgkmcnt(0)\n"
      "v_mfma_f32_16x16x32_bf16 v[100:103], v[76:79], v[92:95], v[100:103]\n"
      "v_cmp_gt_u32 vcc, 16, v254\n"
      "s_nop 1\n"
      "v_cndmask_b32 v245, v253, v245, vcc\n"
      "v_mfma_f32_16x16x32_bf16 v[108:111], v[76:79], v[96:99], v[108:111]\n"
      "ds_read_b128 v[52:55], v11 offset:49152\n"
      "ds_read_b128 v[56:59], v11 offset:50176\n"
      "v_mfma_f32_16x16x32_bf16 v[104:107], v[80:83], v[92:95], v[104:107]\n"
      "buffer_load_dwordx4 v3, s[4:7], s74 offen offset:1024 lds\n"
      "v_mfma_f32_16x16x32_bf16 v[112:115], v[80:83], v[96:99], v[112:115]\n"
      "s_add_u32 m0, s69, 0x4000\n"
      "ds_read_b128 v[60:63], v19 offset:49152\n"
      "ds_read_b128 v[64:67], v19 offset:50176\n"
      "v_mfma_f32_16x16x32_bf16 v[116:119], v[84:87], v[92:95], v[116:119]\n"
      "buffer_load_dwordx4 v2, s[8:11], s82 offen lds\n"
      "v_mfma_f32_16x16x32_bf16 v[124:127], v[84:87], v[96:99], v[124:127]\n"
      "ds_read_b128 v[68:71], v27 offset:49152\n"
      "ds_read_b128 v[72:75], v27 offset:50176\n"
      "v_mfma_f32_16x16x32_bf16 v[120:123], v[88:91], v[92:95], v[120:123]\n"
      "v_mfma_f32_16x16x32_bf16 v[128:131], v[88:91], v[96:99], v[128:131]\n"
      "buffer_load_dwordx4 v3, s[8:11], s82 offen offset:1024 lds\n"
      "v_dot2c_f32_bf16 v34, v92, v248\n"
      "v_dot2c_f32_bf16 v36, v96, v248\n"
      "v_dot2c_f32_bf16 v34, v93, v248\n"
      "v_dot2c_f32_bf16 v36, v97, v248\n"
      "v_dot2c_f32_bf16 v34, v94, v248\n"
      "v_dot2c_f32_bf16 v36, v98, v248\n"
      "v_dot2c_f32_bf16 v34, v95, v248\n"
      "v_dot2c_f32_bf16 v36, v99, v248\n"
      "s_waitcnt lgkmcnt(0)\n"
      "v_mfma_f32_16x16x32_bf16 v[100:103], v[52:55], v[68:71], v[100:103]\n"
      "s_add_u32 m0, s69, 0x6000\n"
      "v_mfma_f32_16x16x32_bf16 v[108:111], v[52:55], v[72:75], v[108:111]\n"
      "s_add_u32 s74, s82, 0x8000\n"
      "ds_read_b128 v[76:79], v12 offset:49152\n"
      "ds_read_b128 v[80:83], v12 offset:50176\n"
      "v_mfma_f32_16x16x32_bf16 v[104:107], v[56:59], v[68:71], v[104:107]\n"
      "buffer_load_dwordx4 v2, s[8:11], s74 offen lds\n"
      "v_mfma_f32_16x16x32_bf16 v[112:115], v[56:59], v[72:75], v[112:115]\n"
      "ds_read_b128 v[84:87], v20 offset:49152\n"
      "ds_read_b128 v[88:91], v20 offset:50176\n"
      "v_mfma_f32_16x16x32_bf16 v[116:119], v[60:63], v[68:71], v[116:119]\n"
      "buffer_load_dwordx4 v3, s[8:11], s74 offen offset:1024 lds\n"
      "v_mfma_f32_16x16x32_bf16 v[124:127], v[60:63], v[72:75], v[124:127]\n"
      "s_add_u32 m0, s69, 0x8000\n"
      "ds_read_b128 v[92:95], v28 offset:49152\n"
      "ds_read_b128 v[96:99], v28 offset:50176\n"
      "v_mfma_f32_16x16x32_bf16 v[120:123], v[64:67], v[68:71], v[120:123]\n"
      "s_add_u32 s74, s82, 0x10000\n"
      "v_mfma_f32_16x16x32_bf16 v[128:131], v[64:67], v[72:75], v[128:131]\n"
      "buffer_load_dwordx4 v2, s[8:11], s74 offen lds\n"
      "v_dot2c_f32_bf16 v38, v52, v248\n"
      "v_dot2c_f32_bf16 v39, v52, v52\n"
      "v_dot2c_f32_bf16 v40, v56, v248\n"
      "v_dot2c_f32_bf16 v41, v56, v56\n"
      "v_dot2c_f32_bf16 v38, v53, v248\n"
      "v_dot2c_f32_bf16 v39, v53, v53\n"
      "v_dot2c_f32_bf16 v40, v57, v248\n"
      "v_dot2c_f32_bf16 v41, v57, v57\n"
      "v_dot2c_f32_bf16 v38, v54, v248\n"
      "v_dot2c_f32_bf16 v39, v54, v54\n"
      "v_dot2c_f32_bf16 v40, v58, v248\n"
      "v_dot2c_f32_bf16 v41, v58, v58\n"
      "v_dot2c_f32_bf16 v38, v55, v248\n"
      "v_dot2c_f32_bf16 v39, v55, v55\n"
      "v_dot2c_f32_bf16 v40, v59, v248\n"
      "v_dot2c_f32_bf16 v41, v59, v59\n"
      "v_dot2c_f32_bf16 v34, v68, v248\n"
      "v_dot2c_f32_bf16 v36, v72, v248\n"
      "v_dot2c_f32_bf16 v34, v69, v248\n"
      "v_dot2c_f32_bf16 v36, v73, v248\n"
      "v_dot2c_f32_bf16 v34, v70, v248\n"
      "v_dot2c_f32_bf16 v36, v74, v248\n"
      "v_dot2c_f32_bf16 v34, v71, v248\n"
      "v_dot2c_f32_bf16 v36, v75, v248\n"
      "s_waitcnt lgkmcnt(0)\n"
      "v_mfma_f32_16x16x32_bf16 v[100:103], v[76:79], v[92:95], v[100:103]\n"
      "s_waitcnt vmcnt(9)\n"
      "s_barrier\n"
      "v_mfma_f32_16x16x32_bf16 v[108:111], v[76:79], v[96:99], v[108:111]\n"
      "ds_read_b128 v[52:55], v13\n"
      "ds_read_b128 v[56:59], v13 offset:1024\n"
      "v_mfma_f32_16x16x32_bf16 v[104:107], v[80:83], v[92:95], v[104:107]\n"
      "buffer_load_dwordx4 v3, s[8:11], s74 offen offset:1024 lds\n"
      "v_mfma_f32_16x16x32_bf16 v[112:115], v[80:83], v[96:99], v[112:115]\n"
      "s_add_u32 m0, s69, 0xa000\n"
      "ds_read_b128 v[60:63], v21\n"
      "ds_read_b128 v[64:67], v21 offset:1024\n"
      "v_mfma_f32_16x16x32_bf16 v[116:119], v[84:87], v[92:95], v[116:119]\n"
      "s_add_u32 s74, s82, 0x18000\n"
      "buffer_load_dwordx4 v2, s[8:11], s74 offen lds\n"
      "v_mfma_f32_16x16x32_bf16 v[124:127], v[84:87], v[96:99], v[124:127]\n"
      "ds_read_b128 v[68:71], v29\n"
      "ds_read_b128 v[72:75], v29 offset:1024\n"
      "v_mfma_f32_16x16x32_bf16 v[120:123], v[88:91], v[92:95], v[120:123]\n"
      "v_mfma_f32_16x16x32_bf16 v[128:131], v[88:91], v[96:99], v[128:131]\n"
      "buffer_load_dwordx4 v3, s[8:11], s74 offen offset:1024 lds\n"
      "v_dot2c_f32_bf16 v34, v92, v248\n"
      "v_dot2c_f32_bf16 v36, v96, v248\n"
      "v_dot2c_f32_bf16 v34, v93, v248\n"
      "v_dot2c_f32_bf16 v36, v97, v248\n"
      "v_dot2c_f32_bf16 v34, v94, v248\n"
      "v_dot2c_f32_bf16 v36, v98, v248\n"
      "v_dot2c_f32_bf16 v34, v95, v248\n"
      "v_dot2c_f32_bf16 v36, v99, v248\n"
      "s_waitcnt lgkmcnt(0)\n"
      "v_mfma_f32_16x16x32_bf16 v[100:103], v[52:55], v[68:71], v[100:103]\n"
      "s_add_u32 m0, s69, 0xc000\n"
      "v_mov_b32 v254, 0x26c00\n"
      "ds_read_b128 v[48:51], v254\n"
      "v_mfma_f32_16x16x32_bf16 v[108:111], v[52:55], v[72:75], v[108:111]\n"
      "ds_read_b128 v[76:79], v14\n"
      "ds_read_b128 v[80:83], v14 offset:1024\n"
      "v_mfma_f32_16x16x32_bf16 v[104:107], v[56:59], v[68:71], v[104:107]\n"
      "buffer_load_dwordx4 v4, s[12:15], s41 offen lds\n"
      "v_mfma_f32_16x16x32_bf16 v[112:115], v[56:59], v[72:75], v[112:115]\n"
      "ds_read_b128 v[84:87], v22\n"
      "ds_read_b128 v[88:91], v22 offset:1024\n"
      "v_mfma_f32_16x16x32_bf16 v[116:119], v[60:63], v[68:71], v[116:119]\n"
      "buffer_load_dwordx4 v5, s[12:15], s41 offen offset:1024 lds\n"
      "v_mfma_f32_16x16x32_bf16 v[124:127], v[60:63], v[72:75], v[124:127]\n"
      "s_add_u32 m0, s69, 0xe000\n"
      "ds_read_b128 v[92:95], v30\n"
      "ds_read_b128 v[96:99], v30 offset:1024\n"
      "v_mfma_f32_16x16x32_bf16 v[120:123], v[64:67], v[68:71], v[120:123]\n"
      "s_add_u32 s74, s41, 0x100\n"
      "s_andn2_b32 s70, s62, 27\n"
      "v_lshl_add_u32 v253, v249, 3, s70\n"
      "v_mfma_f32_16x16x32_bf16 v[128:131], v[64:67], v[72:75], v[128:131]\n"
      "buffer_load_dwordx4 v4, s[12:15], s74 offen lds\n"
      "v_bfe_u32 v252, v0, 2, 2\n"
      "v_add_lshl_u32 v47, v253, v252, 7\n"
      "s_lshl_b32 s70, s62, 7\n"
      "v_dot2c_f32_bf16 v38, v52, v248\n"
      "v_dot2c_f32_bf16 v39, v52, v52\n"
      "v_dot2c_f32_bf16 v40, v56, v248\n"
      "v_dot2c_f32_bf16 v41, v56, v56\n"
      "v_dot2c_f32_bf16 v38, v53, v248\n"
      "v_dot2c_f32_bf16 v39, v53, v53\n"
      "v_dot2c_f32_bf16 v40, v57, v248\n"
      "v_dot2c_f32_bf16 v41, v57, v57\n"
      "v_dot2c_f32_bf16 v38, v54, v248\n"
      "v_dot2c_f32_bf16 v39, v54, v54\n"
      "v_dot2c_f32_bf16 v40, v58, v248\n"
      "v_dot2c_f32_bf16 v41, v58, v58\n"
      "v_dot2c_f32_bf16 v38, v55, v248\n"
      "v_dot2c_f32_bf16 v39, v55, v55\n"
      "v_dot2c_f32_bf16 v40, v59, v248\n"
      "v_dot2c_f32_bf16 v41, v59, v59\n"
      "v_dot2c_f32_bf16 v34, v68, v248\n"
      "v_dot2c_f32_bf16 v36, v72, v248\n"
      "v_dot2c_f32_bf16 v34, v69, v248\n"
      "v_dot2c_f32_bf16 v36, v73, v248\n"
      "v_dot2c_f32_bf16 v34, v70, v248\n"
      "v_dot2c_f32_bf16 v36, v74, v248\n"
      "v_dot2c_f32_bf16 v34, v71, v248\n"
      "v_dot2c_f32_bf16 v36, v75, v248\n"
      "s_waitcnt lgkmcnt(0)\n"
      "v_mfma_f32_16x16x32_bf16 v[100:103], v[76:79], v[92:95], v[100:103]\n"
      "s_add_u32 s70, s70, 0x9000\n"
      "v_sub_u32 v49, v49, v48\n"
      "v_sub_u32 v51, v51, v50\n"
      "v_mfma_f32_16x16x32_bf16 v[108:111], v[76:79], v[96:99], v[108:111]\n"
      "ds_read_b128 v[52:55], v15\n"
      "ds_read_b128 v[56:59], v15 offset:1024\n"
      "v_mfma_f32_16x16x32_bf16 v[104:107], v[80:83], v[92:95], v[104:107]\n"
      "buffer_load_dwordx4 v5, s[12:15], s74 offen offset:1024 lds\n"
      "v_mfma_f32_16x16x32_bf16 v[112:115], v[80:83], v[96:99], v[112:115]\n"
      "s_add_u32 m0, s69, 0x16000\n"
      "ds_read_b128 v[60:63], v23\n"
      "ds_read_b128 v[64:67], v23 offset:1024\n"
      "v_mfma_f32_16x16x32_bf16 v[116:119], v[84:87], v[92:95], v[116:119]\n"
      "s_add_u32 s74, s41, 0x200\n"
      "buffer_load_dwordx4 v4, s[12:15], s74 offen lds\n"
      "v_mfma_f32_16x16x32_bf16 v[124:127], v[84:87], v[96:99], v[124:127]\n"
      "ds_read_b128 v[68:71], v31\n"
      "ds_read_b128 v[72:75], v31 offset:1024\n"
      "v_mfma_f32_16x16x32_bf16 v[120:123], v[88:91], v[92:95], v[120:123]\n"
      "v_add_u32 v49, v49, v51\n"
      "v_alignbit_b32 v48, v246, v49, 31\n"
      "v_mfma_f32_16x16x32_bf16 v[128:131], v[88:91], v[96:99], v[128:131]\n"
      "buffer_load_dwordx4 v5, s[12:15], s74 offen offset:1024 lds\n"
      "v_mov_b32 v49, s61\n"
      "buffer_store_dwordx2 v[48:49], v245, s[32:35], s70 offen sc1\n"
      "v_dot2c_f32_bf16 v34, v92, v248\n"
      "v_dot2c_f32_bf16 v36, v96, v248\n"
      "v_dot2c_f32_bf16 v34, v93, v248\n"
      "v_dot2c_f32_bf16 v36, v97, v248\n"
      "v_dot2c_f32_bf16 v34, v94, v248\n"
      "v_dot2c_f32_bf16 v36, v98, v248\n"
      "v_dot2c_f32_bf16 v34, v95, v248\n"
      "v_dot2c_f32_bf16 v36, v99, v248\n"
      "s_waitcnt lgkmcnt(0)\n"
      "v_mfma_f32_16x16x32_bf16 v[100:103], v[52:55], v[68:71], v[100:103]\n"
      "s_add_u32 m0, s69, 0x10000\n"
      "v_mfma_f32_16x16x32_bf16 v[108:111], v[52:55], v[72:75], v[108:111]\n"
      "ds_read_b128 v[76:79], v16\n"
      "ds_read_b128 v[80:83], v16 offset:1024\n"
      "v_mfma_f32_16x16x32_bf16 v[104:107], v[56:59], v[68:71], v[104:107]\n"
      "buffer_load_dwordx4 v4, s[12:15], s42 offen lds\n"
      "v_mfma_f32_16x16x32_bf16 v[112:115], v[56:59], v[72:75], v[112:115]\n"
      "ds_read_b128 v[84:87], v24\n"
      "ds_read_b128 v[88:91], v24 offset:1024\n"
      "v_mfma_f32_16x16x32_bf16 v[116:119], v[60:63], v[68:71], v[116:119]\n"
      "buffer_load_dwordx4 v5, s[12:15], s42 offen offset:1024 lds\n"
      "v_mfma_f32_16x16x32_bf16 v[124:127], v[60:63], v[72:75], v[124:127]\n"
      "ds_read_b128 v[92:95], v32\n"
      "ds_read_b128 v[96:99], v32 offset:1024\n"
      "v_mfma_f32_16x16x32_bf16 v[120:123], v[64:67], v[68:71], v[120:123]\n"
      "v_mfma_f32_16x16x32_bf16 v[128:131], v[64:67], v[72:75], v[128:131]\n"
      "v_dot2c_f32_bf16 v38, v52, v248\n"
      "v_dot2c_f32_bf16 v39, v52, v52\n"
      "v_dot2c_f32_bf16 v40, v56, v248\n"
      "v_dot2c_f32_bf16 v41, v56, v56\n"
      "v_dot2c_f32_bf16 v38, v53, v248\n"
      "v_dot2c_f32_bf16 v39, v53, v53\n"
      "v_dot2c_f32_bf16 v40, v57, v248\n"
      "v_dot2c_f32_bf16 v41, v57, v57\n"
      "v_dot2c_f32_bf16 v38, v54, v248\n"
      "v_dot2c_f32_bf16 v39, v54, v54\n"
      "v_dot2c_f32_bf16 v40, v58, v248\n"
      "v_dot2c_f32_bf16 v41, v58, v58\n"
      "v_dot2c_f32_bf16 v38, v55, v248\n"
      "v_dot2c_f32_bf16 v39, v55, v55\n"
      "v_dot2c_f32_bf16 v40, v59, v248\n"
      "v_dot2c_f32_bf16 v41, v59, v59\n"
      "v_dot2c_f32_bf16 v34, v68, v248\n"
      "v_dot2c_f32_bf16 v36, v72, v248\n"
      "v_dot2c_f32_bf16 v34, v69, v248\n"
      "v_dot2c_f32_bf16 v36, v73, v248\n"
      "v_dot2c_f32_bf16 v34, v70, v248\n"
      "v_dot2c_f32_bf16 v36, v74, v248\n"
      "v_dot2c_f32_bf16 v34, v71, v248\n"
      "v_dot2c_f32_bf16 v36, v75, v248\n"
      "s_waitcnt lgkmcnt(0)\n"
      "v_mfma_f32_16x16x32_bf16 v[100:103], v[76:79], v[92:95], v[100:103]\n"
      "s_waitcnt vmcnt(9)\n"
      "s_barrier\n"
      "v_mfma_f32_16x16x32_bf16 v[108:111], v[76:79], v[96:99], v[108:111]\n"
      "ds_read_b128 v[52:55], v9\n"
      "ds_read_b128 v[56:59], v9 offset:1024\n"
      "v_mfma_f32_16x16x32_bf16 v[104:107], v[80:83], v[92:95], v[104:107]\n"
      "v_mfma_f32_16x16x32_bf16 v[112:115], v[80:83], v[96:99], v[112:115]\n"
      "ds_read_b128 v[60:63], v17\n"
      "ds_read_b128 v[64:67], v17 offset:1024\n"
      "v_mfma_f32_16x16x32_bf16 v[116:119], v[84:87], v[92:95], v[116:119]\n"
      "v_mfma_f32_16x16x32_bf16 v[124:127], v[84:87], v[96:99], v[124:127]\n"
      "ds_read_b128 v[68:71], v25\n"
      "ds_read_b128 v[72:75], v25 offset:1024\n"
      "v_mfma_f32_16x16x32_bf16 v[120:123], v[88:91], v[92:95], v[120:123]\n"
      "v_mfma_f32_16x16x32_bf16 v[128:131], v[88:91], v[96:99], v[128:131]\n"
      "v_dot2c_f32_bf16 v34, v92, v248\n"
      "v_dot2c_f32_bf16 v36, v96, v248\n"
      "v_dot2c_f32_bf16 v34, v93, v248\n"
      "v_dot2c_f32_bf16 v36, v97, v248\n"
      "v_dot2c_f32_bf16 v34, v94, v248\n"
      "v_dot2c_f32_bf16 v36, v98, v248\n"
      "v_dot2c_f32_bf16 v34, v95, v248\n"
      "v_dot2c_f32_bf16 v36, v99, v248\n"
      "s_waitcnt lgkmcnt(0)\n"
      "v_mfma_f32_16x16x32_bf16 v[100:103], v[52:55], v[68:71], v[100:103]\n"
      "s_add_u32 m0, s69, 0x12000\n"
      "v_mfma_f32_16x16x32_bf16 v[108:111], v[52:55], v[72:75], v[108:111]\n"
      "s_add_u32 s74, s42, 0x100\n"
      "ds_read_b128 v[132:135], v11\n"
      "ds_read_b128 v[136:139], v11 offset:1024\n"
      "v_mfma_f32_16x16x32_bf16 v[104:107], v[56:59], v[68:71], v[104:107]\n"
      "buffer_load_dwordx4 v4, s[12:15], s74 offen lds\n"
      "v_mfma_f32_16x16x32_bf16 v[112:115], v[56:59], v[72:75], v[112:115]\n"
      "ds_read_b128 v[76:79], v10\n"
      "ds_read_b128 v[80:83], v10 offset:1024\n"
      "v_mfma_f32_16x16x32_bf16 v[116:119], v[60:63], v[68:71], v[116:119]\n"
      "buffer_load_dwordx4 v5, s[12:15], s74 offen offset:1024 lds\n"
      "v_mfma_f32_16x16x32_bf16 v[124:127], v[60:63], v[72:75], v[124:127]\n"
      "s_add_u32 m0, s69, 0x24000\n"
      "ds_read_b128 v[84:87], v18\n"
      "ds_read_b128 v[88:91], v18 offset:1024\n"
      "v_mfma_f32_16x16x32_bf16 v[120:123], v[64:67], v[68:71], v[120:123]\n"
      "s_add_u32 s74, s41, 0x400\n"
      "ds_read_b128 v[92:95], v26\n"
      "ds_read_b128 v[96:99], v26 offset:1024\n"
      "v_mfma_f32_16x16x32_bf16 v[128:131], v[64:67], v[72:75], v[128:131]\n"
      "buffer_load_dwordx4 v4, s[12:15], s74 offen lds\n"
      "v_dot2c_f32_bf16 v38, v52, v248\n"
      "v_dot2c_f32_bf16 v39, v52, v52\n"
      "v_dot2c_f32_bf16 v40, v56, v248\n"
      "v_dot2c_f32_bf16 v41, v56, v56\n"
      "v_dot2c_f32_bf16 v38, v53, v248\n"
      "v_dot2c_f32_bf16 v39, v53, v53\n"
      "v_dot2c_f32_bf16 v40, v57, v248\n"
      "v_dot2c_f32_bf16 v41, v57, v57\n"
      "v_dot2c_f32_bf16 v38, v54, v248\n"
      "v_dot2c_f32_bf16 v39, v54, v54\n"
      "v_dot2c_f32_bf16 v40, v58, v248\n"
      "v_dot2c_f32_bf16 v41, v58, v58\n"
      "v_dot2c_f32_bf16 v38, v55, v248\n"
      "v_dot2c_f32_bf16 v39, v55, v55\n"
      "v_dot2c_f32_bf16 v40, v59, v248\n"
      "v_dot2c_f32_bf16 v41, v59, v59\n"
      "s_waitcnt lgkmcnt(7)\n"
      "v_dot2c_f32_bf16 v38, v132, v248\n"
      "v_dot2c_f32_bf16 v39, v132, v132\n"
      "s_waitcnt lgkmcnt(6)\n"
      "v_dot2c_f32_bf16 v40, v136, v248\n"
      "v_dot2c_f32_bf16 v41, v136, v136\n"
      "v_dot2c_f32_bf16 v38, v133, v248\n"
      "v_dot2c_f32_bf16 v39, v133, v133\n"
      "v_dot2c_f32_bf16 v40, v137, v248\n"
      "v_dot2c_f32_bf16 v41, v137, v137\n"
      "v_dot2c_f32_bf16 v38, v134, v248\n"
      "v_dot2c_f32_bf16 v39, v134, v134\n"
      "v_dot2c_f32_bf16 v40, v138, v248\n"
      "v_dot2c_f32_bf16 v41, v138, v138\n"
      "v_dot2c_f32_bf16 v38, v135, v248\n"
      "v_dot2c_f32_bf16 v39, v135, v135\n"
      "v_dot2c_f32_bf16 v40, v139, v248\n"
      "v_dot2c_f32_bf16 v41, v139, v139\n"
      "v_dot2c_f32_bf16 v34, v68, v248\n"
      "v_dot2c_f32_bf16 v36, v72, v248\n"
      "v_dot2c_f32_bf16 v34, v69, v248\n"
      "v_dot2c_f32_bf16 v36, v73, v248\n"
      "v_dot2c_f32_bf16 v34, v70, v248\n"
      "v_dot2c_f32_bf16 v36, v74, v248\n"
      "v_dot2c_f32_bf16 v34, v71, v248\n"
      "v_dot2c_f32_bf16 v36, v75, v248\n"
      "v_permlane32_swap_b32 v38, v40\n"
      "v_permlane32_swap_b32 v39, v41\n"
      "v_add_f32 v38, v38, v40\n"
      "v_add_f32 v39, v39, v41\n"
      "s_nop 1\n"
      "v_permlane16_swap_b32 v38, v39\n"
      "v_add_f32 v38, v38, v39\n"
      "ds_write_b32 v42, v38\n"
      "s_waitcnt lgkmcnt(1)\n"
      "v_mfma_f32_16x16x32_bf16 v[100:103], v[76:79], v[92:95], v[100:103]\n"
      "s_waitcnt lgkmcnt(0)\n"
      "s_barrier\n"
      "ds_read_b32 v140, v43\n"
      "v_mfma_f32_16x16x32_bf16 v[108:111], v[76:79], v[96:99], v[108:111]\n"
      "ds_read_b128 v[52:55], v11\n"
      "ds_read_b128 v[56:59], v11 offset:1024\n"
      "v_mfma_f32_16x16x32_bf16 v[104:107], v[80:83], v[92:95], v[104:107]\n"
      "buffer_load_dwordx4 v5, s[12:15], s74 offen offset:1024 lds\n"
      "v_mfma_f32_16x16x32_bf16 v[112:115], v[80:83], v[96:99], v[112:115]\n"
      "s_add_u32 m0, s69, 0x14000\n"
      "ds_read_b128 v[60:63], v19\n"
      "ds_read_b128 v[64:67], v19 offset:1024\n"
      "v_mfma_f32_16x16x32_bf16 v[116:119], v[84:87], v[92:95], v[116:119]\n"
      "buffer_load_dwordx4 v4, s[12:15], s43 offen lds\n"
      "v_mfma_f32_16x16x32_bf16 v[124:127], v[84:87], v[96:99], v[124:127]\n"
      "ds_read_b128 v[68:71], v27\n"
      "ds_read_b128 v[72:75], v27 offset:1024\n"
      "v_mfma_f32_16x16x32_bf16 v[120:123], v[88:91], v[92:95], v[120:123]\n"
      "ds_read_b32 v141, v43 offset:512\n"
      "ds_read_b32 v142, v43 offset:64\n"
      "v_mfma_f32_16x16x32_bf16 v[128:131], v[88:91], v[96:99], v[128:131]\n"
      "buffer_load_dwordx4 v5, s[12:15], s43 offen offset:1024 lds\n"
      "ds_read_b32 v143, v43 offset:576\n"
      "v_dot2c_f32_bf16 v34, v92, v248\n"
      "v_dot2c_f32_bf16 v36, v96, v248\n"
      "v_dot2c_f32_bf16 v34, v93, v248\n"
      "v_dot2c_f32_bf16 v36, v97, v248\n"
      "v_dot2c_f32_bf16 v34, v94, v248\n"
      "v_dot2c_f32_bf16 v36, v98, v248\n"
      "v_dot2c_f32_bf16 v34, v95, v248\n"
      "v_dot2c_f32_bf16 v36, v99, v248\n"
      "s_waitcnt lgkmcnt(3)\n"
      "v_mfma_f32_16x16x32_bf16 v[100:103], v[52:55], v[68:71], v[100:103]\n"
      "s_add_u32 m0, s69, 0x18000\n"
      "s_waitcnt lgkmcnt(2)\n"
      "v_add_f32 v140, v140, v141\n"
      "v_mfma_f32_16x16x32_bf16 v[108:111], v[52:55], v[72:75], v[108:111]\n"
      "s_add_u32 s74, s41, 0x300\n"
      "ds_read_b128 v[76:79], v12\n"
      "ds_read_b128 v[80:83], v12 offset:1024\n"
      "v_mfma_f32_16x16x32_bf16 v[104:107], v[56:59], v[68:71], v[104:107]\n"
      "buffer_load_dwordx4 v4, s[12:15], s74 offen lds\n"
      "v_mfma_f32_16x16x32_bf16 v[112:115], v[56:59], v[72:75], v[112:115]\n"
      "ds_read_b128 v[84:87], v20\n"
      "ds_read_b128 v[88:91], v20 offset:1024\n"
      "v_mfma_f32_16x16x32_bf16 v[116:119], v[60:63], v[68:71], v[116:119]\n"
      "buffer_load_dwordx4 v5, s[12:15], s74 offen offset:1024 lds\n"
      "v_mfma_f32_16x16x32_bf16 v[124:127], v[60:63], v[72:75], v[124:127]\n"
      "ds_read_b128 v[92:95], v28\n"
      "ds_read_b128 v[96:99], v28 offset:1024\n"
      "v_mfma_f32_16x16x32_bf16 v[120:123], v[64:67], v[68:71], v[120:123]\n"
      "s_waitcnt lgkmcnt(6)\n"
      "v_add_f32 v142, v142, v143\n"
      "v_mul_f32 v140, 0xbb000000, v140\n"
      "v_mfma_f32_16x16x32_bf16 v[128:131], v[64:67], v[72:75], v[128:131]\n"
      "v_mul_f32 v142, 0x3b000000, v142\n"
      "ds_write_b32 v44, v140 offset:256\n"
      "v_fma_f32 v142, -v140, v140, v142\n"
      "v_max_f32 v142, 0, v142\n"
      "v_add_f32 v142, 0x3727c5ac, v142\n"
      "v_dot2c_f32_bf16 v34, v68, v248\n"
      "v_dot2c_f32_bf16 v36, v72, v248\n"
      "v_dot2c_f32_bf16 v34, v69, v248\n"
      "v_dot2c_f32_bf16 v36, v73, v248\n"
      "v_dot2c_f32_bf16 v34, v70, v248\n"
      "v_dot2c_f32_bf16 v36, v74, v248\n"
      "v_dot2c_f32_bf16 v34, v71, v248\n"
      "v_dot2c_f32_bf16 v36, v75, v248\n"
      "s_waitcnt lgkmcnt(1)\n"
      "v_mfma_f32_16x16x32_bf16 v[100:103], v[76:79], v[92:95], v[100:103]\n"
      "v_rsq_f32 v142, v142\n"
      "v_mfma_f32_16x16x32_bf16 v[108:111], v[76:79], v[96:99], v[108:111]\n"
      "v_xor_b32 v142, s53, v142\n"
      "ds_write_b32 v44, v142\n"
      "v_mfma_f32_16x16x32_bf16 v[104:107], v[80:83], v[92:95], v[104:107]\n"
      "ds_read_b128 v[160:163], v45 offset:256\n"
      "ds_read_b128 v[144:147], v45\n"
      "v_mfma_f32_16x16x32_bf16 v[112:115], v[80:83], v[96:99], v[112:115]\n"
      "ds_read_b128 v[164:167], v45 offset:320\n"
      "ds_read_b128 v[148:151], v45 offset:64\n"
      "v_mfma_f32_16x16x32_bf16 v[116:119], v[84:87], v[92:95], v[116:119]\n"
      "ds_read_b128 v[168:171], v46 offset:256\n"
      "ds_read_b128 v[152:155], v46\n"
      "v_mfma_f32_16x16x32_bf16 v[124:127], v[84:87], v[96:99], v[124:127]\n"
      "ds_read_b128 v[172:175], v46 offset:320\n"
      "ds_read_b128 v[156:159], v46 offset:64\n"
      "v_mfma_f32_16x16x32_bf16 v[120:123], v[88:91], v[92:95], v[120:123]\n"
      "v_mfma_f32_16x16x32_bf16 v[128:131], v[88:91], v[96:99], v[128:131]\n"
      "v_dot2c_f32_bf16 v34, v92, v248\n"
      "v_dot2c_f32_bf16 v36, v96, v248\n"
      "v_dot2c_f32_bf16 v34, v93, v248\n"
      "v_dot2c_f32_bf16 v36, v97, v248\n"
      "v_dot2c_f32_bf16 v34, v94, v248\n"
      "v_dot2c_f32_bf16 v36, v98, v248\n"
      "v_dot2c_f32_bf16 v34, v95, v248\n"
      "v_dot2c_f32_bf16 v36, v99, v248\n"
      "s_barrier\n"
      "s_nop 0\n"
      "v_mov_b32 v35, v34\n"
      "v_mov_b32 v37, v36\n"
      "s_nop 0\n"
      "v_permlane32_swap_b32 v34, v35\n"
      "v_permlane32_swap_b32 v36, v37\n"
      "v_add_f32 v34, v34, v35\n"
      "v_add_f32 v36, v36, v37\n"
      "v_mov_b32 v35, v34\n"
      "v_mov_b32 v37, v36\n"
      "s_nop 0\n"
      "v_permlane16_swap_b32 v34, v35\n"
      "v_permlane16_swap_b32 v36, v37\n"
      "v_add_f32 v34, v34, v35\n"
      "v_add_f32 v36, v36, v37\n"
      "v_mov_b32 v35, v34\n"
      "v_mov_b32 v37, v36\n"
      "s_waitcnt lgkmcnt(7)\n"
      "v_pk_fma_f32 v[100:101], v[160:161], v[34:35], v[100:101]\n"
      "v_pk_fma_f32 v[102:103], v[162:163], v[34:35], v[102:103]\n"
      "s_waitcnt lgkmcnt(5)\n"
      "v_pk_fma_f32 v[104:105], v[164:165], v[34:35], v[104:105]\n"
      "v_pk_fma_f32 v[106:107], v[166:167], v[34:35], v[106:107]\n"
      "v_pk_mul_f32 v[100:101], v[100:101], v[144:145]\n"
      "v_pk_mul_f32 v[102:103], v[102:103], v[146:147]\n"
      "s_waitcnt lgkmcnt(4)\n"
      "v_pk_mul_f32 v[104:105], v[104:105], v[148:149]\n"
      "v_pk_mul_f32 v[106:107], v[106:107], v[150:151]\n"
      "v_cvt_pk_bf16_f32 v10, v100, v101\n"
      "v_cvt_pk_bf16_f32 v11, v102, v103\n"
      "v_cvt_pk_bf16_f32 v12, v104, v105\n"
      "v_cvt_pk_bf16_f32 v13, v106, v107\n"
      "buffer_store_dwordx4 v[10:13], v255, s[32:35], s47 offen sc1\n"
      "v_pk_fma_f32 v[108:109], v[160:161], v[36:37], v[108:109]\n"
      "v_pk_fma_f32 v[110:111], v[162:163], v[36:37], v[110:111]\n"
      "v_pk_fma_f32 v[112:113], v[164:165], v[36:37], v[112:113]\n"
      "v_pk_fma_f32 v[114:115], v[166:167], v[36:37], v[114:115]\n"
      "v_pk_mul_f32 v[108:109], v[108:109], v[144:145]\n"
      "v_pk_mul_f32 v[110:111], v[110:111], v[146:147]\n"
      "v_pk_mul_f32 v[112:113], v[112:113], v[148:149]\n"
      "v_pk_mul_f32 v[114:115], v[114:115], v[150:151]\n"
      "v_cvt_pk_bf16_f32 v14, v108, v109\n"
      "v_cvt_pk_bf16_f32 v15, v110, v111\n"
      "v_cvt_pk_bf16_f32 v16, v112, v113\n"
      "v_cvt_pk_bf16_f32 v17, v114, v115\n"
      "buffer_store_dwordx4 v[14:17], v255, s[32:35], s47 offen offset:1024 sc1\n"
      "s_waitcnt lgkmcnt(3)\n"
      "v_pk_fma_f32 v[116:117], v[168:169], v[34:35], v[116:117]\n"
      "v_pk_fma_f32 v[118:119], v[170:171], v[34:35], v[118:119]\n"
      "s_waitcnt lgkmcnt(1)\n"
      "v_pk_fma_f32 v[120:121], v[172:173], v[34:35], v[120:121]\n"
      "v_pk_fma_f32 v[122:123], v[174:175], v[34:35], v[122:123]\n"
      "v_pk_mul_f32 v[116:117], v[116:117], v[152:153]\n"
      "v_pk_mul_f32 v[118:119], v[118:119], v[154:155]\n"
      "s_waitcnt lgkmcnt(0)\n"
      "v_pk_mul_f32 v[120:121], v[120:121], v[156:157]\n"
      "v_pk_mul_f32 v[122:123], v[122:123], v[158:159]\n"
      "v_cvt_pk_bf16_f32 v18, v116, v117\n"
      "v_cvt_pk_bf16_f32 v19, v118, v119\n"
      "v_cvt_pk_bf16_f32 v20, v120, v121\n"
      "v_cvt_pk_bf16_f32 v21, v122, v123\n"
      "buffer_store_dwordx4 v[18:21], v255, s[32:35], s52 offen sc1\n"
      "v_pk_fma_f32 v[124:125], v[168:169], v[36:37], v[124:125]\n"
      "v_pk_fma_f32 v[126:127], v[170:171], v[36:37], v[126:127]\n"
      "v_pk_fma_f32 v[128:129], v[172:173], v[36:37], v[128:129]\n"
      "v_pk_fma_f32 v[130:131], v[174:175], v[36:37], v[130:131]\n"
      "v_pk_mul_f32 v[124:125], v[124:125], v[152:153]\n"
      "v_pk_mul_f32 v[126:127], v[126:127], v[154:155]\n"
      "v_pk_mul_f32 v[128:129], v[128:129], v[156:157]\n"
      "v_pk_mul_f32 v[130:131], v[130:131], v[158:159]\n"
      "v_cvt_pk_bf16_f32 v22, v124, v125\n"
      "v_cvt_pk_bf16_f32 v23, v126, v127\n"
      "v_cvt_pk_bf16_f32 v24, v128, v129\n"
      "v_cvt_pk_bf16_f32 v25, v130, v131\n"
      "buffer_store_dwordx4 v[22:25], v255, s[32:35], s52 offen offset:1024 sc1\n"
      "s_add_u32 m0, s69, 0x1e000\n"
      "s_add_u32 s74, s43, 0x100\n"
      "buffer_load_dwordx4 v4, s[12:15], s74 offen lds\n"
      "buffer_load_dwordx4 v5, s[12:15], s74 offen offset:1024 lds\n"
      "s_add_u32 m0, s69, 0x20000\n"
      "s_add_u32 s74, s41, 0x500\n"
      "buffer_load_dwordx4 v4, s[12:15], s74 offen lds\n"
      "buffer_load_dwordx4 v5, s[12:15], s74 offen offset:1024 lds\n"
      "s_lshl_b32 s8, s62, 7\n"
      "s_add_u32 s8, s8, 0x1000\n"
      "s_mov_b32 s73, 0\n"
      "s_mov_b64 s[48:49], s[32:33]\n"
      "s_mov_b64 s[50:51], s[34:35]\n"
      "s_lshl2_add_u32 s10, s65, s64\n"
      "s_lshl_b32 s9, s10, 7\n"
      "s_add_u32 s9, s9, s45\n"
      "s_lshl_b32 s70, s64, 1\n"
      "s_lshl3_add_u32 s70, s65, s70\n"
      "v_lshrrev_b32 v254, 5, v0\n"
      "v_add_u32 v254, s70, v254\n"
      "v_bfe_u32 v253, v0, 2, 3\n"
      "v_lshl_add_u32 v254, v253, 5, v254\n"
      "v_lshlrev_b32 v244, 7, v254\n"
      "v_add_u32 v244, 0x1000, v244\n"
      "v_lshl_add_u32 v253, s65, 2, v249\n"
      "v_lshl_add_u32 v241, v253, 7, s45\n"
      "s_add_u32 s70, s64, 1\n"
      "s_and_b32 s70, s70, 3\n"
      "s_lshl2_add_u32 s70, s65, s70\n"
      "s_lshl2_add_u32 s70, s70, s63\n"
      "s_lshl_b32 s70, s70, 12\n"
      "s_andn2_b32 s74, s70, 0x1fff\n"
      "s_add_u32 s70, s70, s74\n"
      "s_add_u32 s70, s70, s45\n"
      "s_add_u32 s11, s70, 0\n"
      "s_add_u32 s70, s64, 2\n"
      "s_and_b32 s70, s70, 3\n"
      "s_lshl2_add_u32 s70, s65, s70\n"
      "s_lshl2_add_u32 s70, s70, s63\n"
      "s_lshl_b32 s70, s70, 12\n"
      "s_add_u32 s70, s70, 0x800\n"
      "s_andn2_b32 s74, s70, 0x1fff\n"
      "s_add_u32 s70, s70, s74\n"
      "s_add_u32 s70, s70, s45\n"
      "s_add_u32 s46, s70, 0\n"
      "s_add_u32 s70, s64, 3\n"
      "s_and_b32 s70, s70, 3\n"
      "s_lshl2_add_u32 s70, s65, s70\n"
      "s_lshl2_add_u32 s70, s70, s63\n"
      "s_lshl_b32 s70, s70, 11\n"
      "s_andn2_b32 s74, s70, 0x1fff\n"
      "s_add_u32 s70, s70, s74\n"
      "s_add_u32 s70, s70, s45\n"
      "s_add_u32 s47, s70, 0x80000\n"
      "v_sub_u32 v254, s63, v249\n"
      "v_and_b32 v254, 3, v254\n"
      "v_lshl_add_u32 v254, v249, 2, v254\n"
      "v_lshl_add_u32 v240, v254, 7, s45\n"
      "v_mov_b32 v26, -1\n"
      "v_mov_b32 v27, -1\n"
      "v_mov_b32 v28, -1\n"
      "v_mov_b32 v29, -1\n"
      "v_xor_b32 v30, 0, v8\n"
      "v_add_u32 v34, 0x10000, v30\n"
      "v_add_u32 v38, 0x20000, v30\n"
      "v_xor_b32 v31, 64, v8\n"
      "v_add_u32 v35, 0x10000, v31\n"
      "v_add_u32 v39, 0x20000, v31\n"
      "v_xor_b32 v32, 0x80, v8\n"
      "v_add_u32 v36, 0x10000, v32\n"
      "v_add_u32 v40, 0x20000, v32\n"
      "v_xor_b32 v33, 0xc0, v8\n"
      "v_add_u32 v37, 0x10000, v33\n"
      "v_add_u32 v41, 0x20000, v33\n"
      "s_lshl_b32 s70, s63, 6\n"
      "v_xor_b32 v9, s70, v8\n"
      "v_lshlrev_b32 v254, 2, v249\n"
      "v_lshl_add_u32 v42, v6, 4, v254\n"
      "s_lshr_b32 s70, s64, 1\n"
      "s_lshl2_add_u32 s70, s70, s65\n"
      "s_lshl2_add_u32 s70, s70, s63\n"
      "s_lshl_b32 s70, s70, 17\n"
      "s_and_b32 s71, s64, 1\n"
      "s_lshl_b32 s71, s71, 16\n"
      "s_add_u32 s70, s70, s71\n"
      "s_add_u32 s52, s70, 0x100000\n"
      "s_lshl2_add_u32 s71, s10, s63\n"
      "s_lshl_b32 s70, s71, 12\n"
      "s_andn2_b32 s74, s70, 0x1fff\n"
      "s_add_u32 s70, s70, s74\n"
      "s_add_u32 s70, s70, s45\n"
      "s_add_u32 s53, s70, 0\n"
      "s_lshl_b32 s70, s71, 11\n"
      "s_andn2_b32 s74, s70, 0x1fff\n"
      "s_add_u32 s70, s70, s74\n"
      "s_add_u32 s70, s70, s45\n"
      "s_add_u32 s54, s70, 0x80000\n"
      "s_lshl2_add_u32 s70, s68, s40\n"
      "s_lshl_b32 s71, s70, 15\n"
      "s_lshl_b32 s72, s65, 8\n"
      "s_add_u32 s55, s71, s72\n"
      "s_lshl2_add_u32 s71, s40, s65\n"
      "s_lshl2_add_u32 s71, s71, s63\n"
      "s_lshl_b32 s70, s71, 11\n"
      "s_andn2_b32 s74, s70, 0x1fff\n"
      "s_add_u32 s70, s70, s74\n"
      "s_add_u32 s70, s70, s45\n"
      "s_add_u32 s56, s70, 0xc0000\n"
      "s_lshl_b32 s70, s71, 8\n"
      "s_andn2_b32 s74, s70, 0x1fff\n"
      "s_add_u32 s70, s70, s74\n"
      "s_add_u32 s70, s70, s45\n"
      "s_add_u32 s57, s70, 0x100000\n"
      "s_waitcnt vmcnt(4)\n"
      "s_barrier\n"
      "s_add_u32 s70, s8, 0\n"
      "buffer_store_dwordx2 v[246:247], v245, s[32:35], s70 offen sc1\n"
      "buffer_load_dword v251, v250, s[36:39], 0 offen\n"
      "s_add_u32 m0, s69, 0\n"
      "s_add_u32 s74, s41, 0x600\n"
      "buffer_load_dwordx4 v4, s[12:15], s74 offen lds\n"
      "buffer_load_dwordx4 v5, s[12:15], s74 offen offset:1024 lds\n"
      "s_add_u32 m0, s69, 0x2000\n"
      "s_add_u32 s74, s41, 0x700\n"
      "buffer_load_dwordx4 v4, s[12:15], s74 offen lds\n"
      "buffer_load_dwordx4 v5, s[12:15], s74 offen offset:1024 lds\n"
      "s_add_u32 m0, s69, 0x1a000\n"
      "s_add_u32 s74, s42, 0x200\n"
      "buffer_load_dwordx4 v4, s[12:15], s74 offen lds\n"
      "buffer_load_dwordx4 v5, s[12:15], s74 offen offset:1024 lds\n"
      "s_lshl2_add_u32 s40, s65, s64\n"
      "s_mul_i32 s70, s40, 3\n"
      "s_lshl1_add_u32 s72, s40, 11\n"
      "s_min_u32 s70, s70, s72\n"
      "s_mul_i32 s71, s63, 0xac00\n"
      "s_lshl_b32 s72, s70, 6\n"
      "s_add_u32 s58, s71, s72\n"
      "s_mul_i32 s59, s63, 0x1800\n"
      "s_cmp_ge_u32 s40, 11\n"
      "s_cselect_b32 s70, 8, 12\n"
      "s_cselect_b32 s71, 0x40000000, 0\n"
      "v_add_u32 v233, 0x1000, v255\n"
      "v_or_b32 v234, s71, v255\n"
      "v_or_b32 v235, s71, v233\n"
      "v_add_u32 v254, 0, v0\n"
      "v_mul_u32_u24 v253, 0x1556, v254\n"
      "v_lshrrev_b32 v253, 16, v253\n"
      "v_mul_u32_u24 v252, 12, v253\n"
      "v_sub_u32 v254, v254, v252\n"
      "v_cmp_gt_u32 vcc, s70, v254\n"
      "v_lshrrev_b32 v252, 3, v253\n"
      "v_lshlrev_b32 v252, 1, v252\n"
      "v_xor_b32 v254, v254, v252\n"
      "v_mul_u32_u24 v253, 0xac0, v253\n"
      "v_lshl_add_u32 v254, v254, 4, v253\n"
      "v_mov_b32 v253, 0x40000000\n"
      "v_cndmask_b32 v236, v253, v254, vcc\n"
      "v_add_u32 v254, 64, v0\n"
      "v_mul_u32_u24 v253, 0x1556, v254\n"
      "v_lshrrev_b32 v253, 16, v253\n"
      "v_mul_u32_u24 v252, 12, v253\n"
      "v_sub_u32 v254, v254, v252\n"
      "v_cmp_gt_u32 vcc, s70, v254\n"
      "v_lshrrev_b32 v252, 3, v253\n"
      "v_lshlrev_b32 v252, 1, v252\n"
      "v_xor_b32 v254, v254, v252\n"
      "v_mul_u32_u24 v253, 0xac0, v253\n"
      "v_lshl_add_u32 v254, v254, 4, v253\n"
      "v_subrev_u32 v254, 0x400, v254\n"
      "v_mov_b32 v253, 0x40000000\n"
      "v_cndmask_b32 v237, v253, v254, vcc\n"
      "v_add_u32 v254, 0x80, v0\n"
      "v_mul_u32_u24 v253, 0x1556, v254\n"
      "v_lshrrev_b32 v253, 16, v253\n"
      "v_mul_u32_u24 v252, 12, v253\n"
      "v_sub_u32 v254, v254, v252\n"
      "v_cmp_gt_u32 vcc, s70, v254\n"
      "v_lshrrev_b32 v252, 3, v253\n"
      "v_lshlrev_b32 v252, 1, v252\n"
      "v_xor_b32 v254, v254, v252\n"
      "v_mul_u32_u24 v253, 0xac0, v253\n"
      "v_lshl_add_u32 v254, v254, 4, v253\n"
      "v_subrev_u32 v254, 0x800, v254\n"
      "v_mov_b32 v253, 0x40000000\n"
      "v_cndmask_b32 v238, v253, v254, vcc\n"
      "s_add_u32 m0, s69, 0xa000\n"
      "s_add_u32 s74, s43, 0x200\n"
      "buffer_load_dwordx4 v4, s[12:15], s74 offen lds\n"
      "buffer_load_dwordx4 v5, s[12:15], s74 offen offset:1024 lds\n"
      "s_add_u32 m0, s69, 0x22000\n"
      "s_add_u32 s74, s42, 0x400\n"
      "buffer_load_dwordx4 v4, s[12:15], s74 offen lds\n"
      "buffer_load_dwordx4 v5, s[12:15], s74 offen offset:1024 lds\n"
      "v_bfe_u32 v8, v0, 2, 2\n"
      "v_and_b32 v10, 3, v0\n"
      "v_and_b32 v252, 1, v249\n"
      "v_lshl_add_u32 v254, v252, 3, v8\n"
      "v_mul_u32_u24 v254, 0xc0, v254\n"
      "v_lshl_add_u32 v254, v10, 3, v254\n"
      "v_lshl_add_u32 v254, v252, 5, v254\n"
      "v_lshrrev_b32 v253, 1, v249\n"
      "v_mul_u32_u24 v230, 0x1800, v253\n"
      "v_add_u32 v230, v230, v254\n"
      "v_xor_b32 v227, 32, v230\n"
      "v_lshl_add_u32 v254, v253, 3, v8\n"
      "v_mul_u32_u24 v254, 0xc0, v254\n"
      "v_lshl_add_u32 v254, v10, 3, v254\n"
      "v_xor_b32 v253, v253, v252\n"
      "v_lshl_add_u32 v254, v253, 5, v254\n"
      "v_add_u32 v224, s59, v254\n"
      "v_add_u32 v231, 0xc000, v230\n"
      "v_add_u32 v228, 0xc000, v227\n"
      "v_add_u32 v225, 0xc000, v224\n"
      "v_add_u32 v232, 0x18000, v230\n"
      "v_add_u32 v229, 0x18000, v227\n"
      "v_add_u32 v226, 0x18000, v224\n"
      "v_lshlrev_b32 v254, 4, v10\n"
      "v_lshl_add_u32 v254, v8, 6, v254\n"
      "v_lshl_add_u32 v254, v249, 9, v254\n"
      "s_add_u32 s71, s59, 0xc000\n"
      "v_add_u32 v223, s71, v254\n"
      "s_add_u32 s71, s69, 0x24000\n"
      "v_add_u32 v222, s71, v254\n"
      "s_lshl_b32 s70, s63, 15\n"
      "s_andn2_b32 s74, s70, 0x1fff\n"
      "s_add_u32 s70, s70, s74\n"
      "s_add_u32 s70, s70, s45\n"
      "s_add_u32 s75, s70, 0xc0000\n"
      "s_lshl_b32 s70, s63, 12\n"
      "s_andn2_b32 s74, s70, 0x1fff\n"
      "s_add_u32 s70, s70, s74\n"
      "s_add_u32 s70, s70, s45\n"
      "s_add_u32 s76, s70, 0x100000\n"
      "s_mul_i32 s71, s63, 0x60\n"
      "s_lshl3_add_u32 s72, s65, s64\n"
      "s_add_u32 s70, s71, s72\n"
      "s_lshl_b32 s70, s70, 10\n"
      "s_andn2_b32 s74, s70, 0x1fff\n"
      "s_add_u32 s70, s70, s74\n"
      "s_add_u32 s70, s70, s45\n"
      "s_add_u32 s77, s70, 0x120000\n"
      "s_add_u32 s78, s77, 0x10000\n"
      "s_mul_i32 s72, s40, 43\n"
      "s_lshr_b32 s72, s72, 7\n"
      "s_mul_i32 s72, s72, 5\n"
      "s_add_u32 s72, s72, s40\n"
      "s_add_u32 s70, s71, s72\n"
      "s_lshl_b32 s70, s70, 10\n"
      "s_andn2_b32 s74, s70, 0x1fff\n"
      "s_add_u32 s70, s70, s74\n"
      "s_add_u32 s70, s70, s45\n"
      "s_add_u32 s79, s70, 0x140000\n"
      "s_add_u32 m0, s69, 0x1c000\n"
      "s_add_u32 s74, s42, 0x300\n"
      "buffer_load_dwordx4 v4, s[12:15], s74 offen lds\n"
      "buffer_load_dwordx4 v5, s[12:15], s74 offen offset:1024 lds\n"
      "s_and_b32 s70, s64, 1\n"
      "s_lshl2_add_u32 s40, s70, s65\n"
      "s_lshl_b32 s84, s40, 7\n"
      "s_lshl2_add_u32 s70, s63, 0\n"
      "s_mul_i32 s71, s70, 3\n"
      "s_lshl1_add_u32 s72, s70, 11\n"
      "s_min_u32 s71, s71, s72\n"
      "s_lshl_b32 s71, s71, 15\n"
      "s_add_u32 s80, s71, s84\n"
      "s_lshl2_add_u32 s70, s63, 1\n"
      "s_mul_i32 s71, s70, 3\n"
      "s_lshl1_add_u32 s72, s70, 11\n"
      "s_min_u32 s71, s71, s72\n"
      "s_lshl_b32 s71, s71, 15\n"
      "s_add_u32 s81, s71, s84\n"
      "s_lshl2_add_u32 s70, s63, 2\n"
      "s_mul_i32 s71, s70, 3\n"
      "s_lshl1_add_u32 s72, s70, 11\n"
      "s_min_u32 s71, s71, s72\n"
      "s_lshl_b32 s71, s71, 15\n"
      "s_add_u32 s82, s71, s84\n"
      "s_lshl2_add_u32 s70, s63, 3\n"
      "s_mul_i32 s71, s70, 3\n"
      "s_lshl1_add_u32 s72, s70, 11\n"
      "s_min_u32 s71, s71, s72\n"
      "s_lshl_b32 s71, s71, 15\n"
      "s_add_u32 s83, s71, s84\n"
      "s_mul_i32 s71, s63, 0x48000\n"
      "s_add_u32 s84, s84, s71\n"
      "v_lshrrev_b32 v254, 5, v0\n"
      "v_lshl_add_u32 v254, v254, 2, v8\n"
      "v_xor_b32 v253, s63, v249\n"
      "v_and_b32 v253, 1, v253\n"
      "v_lshl_add_u32 v253, v253, 2, v10\n"
      "v_lshlrev_b32 v253, 4, v253\n"
      "v_lshl_add_u32 v218, v254, 10, v253\n"
      "v_add_u32 v219, 0x1c00, v218\n"
      "s_cmp_eq_u32 s63, 3\n"
      "s_cselect_b32 s71, 0x40000000, 0\n"
      "v_or_b32 v220, s71, v218\n"
      "v_or_b32 v221, s71, v219\n"
      "v_or_b32 v217, s71, v255\n"
      "v_lshlrev_b32 v254, 4, v249\n"
      "v_lshl_add_u32 v216, v7, 10, v254\n"
      "s_and_b32 s71, s64, 2\n"
      "s_lshr_b32 s70, s63, 1\n"
      "s_add_u32 s10, s70, s71\n"
      "s_and_b32 s72, s63, 1\n"
      "s_lshl1_add_u32 s71, s40, s72\n"
      "s_lshl2_add_u32 s72, s68, s10\n"
      "s_lshl_b32 s72, s72, 15\n"
      "s_lshl_b32 s85, s71, 6\n"
      "s_add_u32 s85, s85, s72\n"
      "s_lshl4_add_u32 s70, s10, s71\n"
      "s_lshl_b32 s70, s70, 11\n"
      "s_andn2_b32 s74, s70, 0x1fff\n"
      "s_add_u32 s70, s70, s74\n"
      "s_add_u32 s70, s70, s45\n"
      "s_add_u32 s86, s70, 0xc0000\n"
      "s_xor_b32 s71, s10, 0\n"
      "s_mul_i32 s71, s71, 0x18000\n"
      "s_lshl2_add_u32 s70, s69, s71\n"
      "s_andn2_b32 s74, s70, 0x1fff\n"
      "s_add_u32 s70, s70, s74\n"
      "s_add_u32 s70, s70, s45\n"
      "s_add_u32 s87, s70, 0x120000\n"
      "s_xor_b32 s71, s10, 1\n"
      "s_mul_i32 s71, s71, 0x18000\n"
      "s_lshl2_add_u32 s70, s69, s71\n"
      "s_andn2_b32 s74, s70, 0x1fff\n"
      "s_add_u32 s70, s70, s74\n"
      "s_add_u32 s70, s70, s45\n"
      "s_add_u32 s88, s70, 0x120000\n"
      "s_mov_b32 s72, 0\n"
      "buffer_load_dwordx2 v[242:243], v244, s[32:35], s72 offen sc1\n"
      "s_mov_b32 s72, 0x9000\n"
      "buffer_load_dwordx2 v[6:7], v47, s[32:35], s72 offen sc1\n"
      "s_add_u32 m0, s69, 0x4000\n"
      "s_add_u32 s74, s42, 0x500\n"
      "buffer_load_dwordx4 v4, s[12:15], s74 offen lds\n"
      "buffer_load_dwordx4 v5, s[12:15], s74 offen offset:1024 lds\n"
      "s_add_u32 m0, s69, 0x6000\n"
      "s_add_u32 s74, s42, 0x600\n"
      "buffer_load_dwordx4 v4, s[12:15], s74 offen lds\n"
      "buffer_load_dwordx4 v5, s[12:15], s74 offen offset:1024 lds\n"
      "s_add_u32 m0, s69, 0x8000\n"
      "s_add_u32 s74, s42, 0x700\n"
      "buffer_load_dwordx4 v4, s[12:15], s74 offen lds\n"
      "buffer_load_dwordx4 v5, s[12:15], s74 offen offset:1024 lds\n"
      "s_waitcnt vmcnt(7)\n"
      "v_cmp_ne_u64 vcc, s[60:61], v[242:243]\n"
      "s_nop 0\n"
      "s_cbranch_vccnz L1_%=\n"
      "L2_%=:\n"
      "buffer_load_dwordx4 a[0:3], v255, s[32:35], s52 offen sc1\n"
      "buffer_load_dwordx4 a[4:7], v255, s[32:35], s52 offen offset:1024 sc1\n"
      "buffer_load_dwordx4 a[8:11], v255, s[32:35], s52 offen offset:2048 sc1\n"
      "buffer_load_dwordx4 a[12:15], v255, s[32:35], s52 offen offset:3072 sc1\n"
      "s_add_u32 s52, s52, 0x1000\n"
      "buffer_load_dwordx4 a[16:19], v255, s[32:35], s52 offen sc1\n"
      "buffer_load_dwordx4 a[20:23], v255, s[32:35], s52 offen offset:1024 sc1\n"
      "buffer_load_dwordx4 a[24:27], v255, s[32:35], s52 offen offset:2048 sc1\n"
      "buffer_load_dwordx4 a[28:31], v255, s[32:35], s52 offen offset:3072 sc1\n"
      "v_lshlrev_b32 v254, 1, v246\n"
      "s_waitcnt vmcnt(14)\n"
      "v_xor_b32 v254, v254, v6\n"
      "v_xor_b32 v253, v247, v7\n"
      "v_and_or_b32 v254, v254, -2, v253\n"
      "v_cmp_ne_u32 vcc, 0, v254\n"
      "s_nop 0\n"
      "s_cbranch_vccnz L3_%=\n"
      "L4_%=:\n"
      "v_and_b32 v254, 1, v6\n"
      "v_cmp_eq_u32 vcc, 1, v254\n"
      "s_nop 0\n"
      "s_bcnt1_i32_b64 s70, vcc\n"
      "s_cmp_gt_u32 s70, 32\n"
      "s_cselect_b32 s70, 0x2000, 0\n"
      "s_add_u32 s48, s48, s70\n"
      "s_addc_u32 s49, s49, 0\n"
      "s_add_u32 s52, s52, 0x1000\n"
      "buffer_load_dwordx4 a[32:35], v255, s[32:35], s52 offen sc1\n"
      "buffer_load_dwordx4 a[36:39], v255, s[32:35], s52 offen offset:1024 sc1\n"
      "buffer_load_dwordx4 a[40:43], v255, s[32:35], s52 offen offset:2048 sc1\n"
      "buffer_load_dwordx4 a[44:47], v255, s[32:35], s52 offen offset:3072 sc1\n"
      "s_add_u32 s52, s52, 0x1000\n"
      "buffer_load_dwordx4 a[48:51], v255, s[32:35], s52 offen sc1\n"
      "buffer_load_dwordx4 a[52:55], v255, s[32:35], s52 offen offset:1024 sc1\n"
      "buffer_load_dwordx4 a[56:59], v255, s[32:35], s52 offen offset:2048 sc1\n"
      "buffer_load_dwordx4 a[60:63], v255, s[32:35], s52 offen offset:3072 sc1\n"
      "s_waitcnt vmcnt(16)\n"
      "s_barrier\n"
      "ds_read_b128 v[96:99], v30 offset:49152\n"
      "ds_read_b128 v[100:103], v30 offset:50176\n"
      "ds_read_b128 v[104:107], v34\n"
      "ds_read_b128 v[108:111], v34 offset:1024\n"
      "ds_read_b128 v[112:115], v34 offset:16384\n"
      "ds_read_b128 v[116:119], v31 offset:49152\n"
      "ds_read_b128 v[120:123], v31 offset:50176\n"
      "s_waitcnt vmcnt(14) lgkmcnt(5)\n"
      "v_mfma_f32_16x16x32_bf16 v[10:13], a[0:3], v[96:99], 0\n"
      "s_add_u32 s52, s52, 0x1000\n"
      "buffer_load_dwordx4 a[64:67], v255, s[32:35], s52 offen sc1\n"
      "v_mfma_f32_16x16x32_bf16 v[14:17], a[4:7], v[96:99], 0\n"
      "ds_read_b128 v[124:127], v35\n"
      "v_mfma_f32_16x16x32_bf16 v[18:21], a[0:3], v[100:103], 0\n"
      "v_mfma_f32_16x16x32_bf16 v[22:25], a[4:7], v[100:103], 0\n"
      "s_waitcnt lgkmcnt(4)\n"
      "ds_read_b128 v[96:99], v35 offset:1024\n"
      "v_mfma_f32_16x16x32_bf16 v[48:51], a[0:3], v[104:107], 0\n"
      "buffer_load_dwordx4 a[68:71], v255, s[32:35], s52 offen offset:1024 sc1\n"
      "v_mfma_f32_16x16x32_bf16 v[52:55], a[4:7], v[104:107], 0\n"
      "ds_read_b128 v[100:103], v34 offset:17408\n"
      "v_mfma_f32_16x16x32_bf16 v[56:59], a[0:3], v[108:111], 0\n"
      "v_mfma_f32_16x16x32_bf16 v[60:63], a[4:7], v[108:111], 0\n"
      "s_waitcnt vmcnt(14) lgkmcnt(4)\n"
      "ds_read_b128 v[104:107], v35 offset:16384\n"
      "v_mfma_f32_16x16x32_bf16 v[64:67], a[0:3], v[112:115], 0\n"
      "buffer_store_dwordx4 v[26:29], v255, s[48:51], s11 offen sc1\n"
      "v_mfma_f32_16x16x32_bf16 v[68:71], a[4:7], v[112:115], 0\n"
      "ds_read_b128 v[108:111], v32 offset:49152\n"
      "v_mfma_f32_16x16x32_bf16 v[10:13], a[8:11], v[116:119], v[10:13]\n"
      "v_mfma_f32_16x16x32_bf16 v[14:17], a[12:15], v[116:119], v[14:17]\n"
      "s_waitcnt lgkmcnt(4)\n"
      "ds_read_b128 v[112:115], v32 offset:50176\n"
      "v_mfma_f32_16x16x32_bf16 v[18:21], a[8:11], v[120:123], v[18:21]\n"
      "v_mfma_f32_16x16x32_bf16 v[22:25], a[12:15], v[120:123], v[22:25]\n"
      "ds_read_b128 v[116:119], v36\n"
      "v_mfma_f32_16x16x32_bf16 v[48:51], a[8:11], v[124:127], v[48:51]\n"
      "buffer_load_dwordx4 a[72:75], v255, s[32:35], s52 offen offset:2048 sc1\n"
      "v_mfma_f32_16x16x32_bf16 v[52:55], a[12:15], v[124:127], v[52:55]\n"
      "s_waitcnt lgkmcnt(4)\n"
      "ds_read_b128 v[120:123], v36 offset:1024\n"
      "v_mfma_f32_16x16x32_bf16 v[56:59], a[8:11], v[96:99], v[56:59]\n"
      "v_mfma_f32_16x16x32_bf16 v[60:63], a[12:15], v[96:99], v[60:63]\n"
      "ds_read_b128 v[124:127], v35 offset:17408\n"
      "v_mfma_f32_16x16x32_bf16 v[72:75], a[0:3], v[100:103], 0\n"
      "buffer_load_dwordx4 a[76:79], v255, s[32:35], s52 offen offset:3072 sc1\n"
      "v_mfma_f32_16x16x32_bf16 v[76:79], a[4:7], v[100:103], 0\n"
      "s_waitcnt vmcnt(15) lgkmcnt(4)\n"
      "ds_read_b128 v[96:99], v33 offset:49152\n"
      "s_add_u32 s52, s52, 0x1000\n"
      "v_mfma_f32_16x16x32_bf16 v[64:67], a[8:11], v[104:107], v[64:67]\n"
      "v_mfma_f32_16x16x32_bf16 v[68:71], a[12:15], v[104:107], v[68:71]\n"
      "ds_read_b128 v[100:103], v33 offset:50176\n"
      "v_mfma_f32_16x16x32_bf16 v[10:13], a[16:19], v[108:111], v[10:13]\n"
      "buffer_load_dwordx4 a[80:83], v255, s[32:35], s52 offen sc1\n"
      "v_mfma_f32_16x16x32_bf16 v[14:17], a[20:23], v[108:111], v[14:17]\n"
      "s_waitcnt lgkmcnt(4)\n"
      "ds_read_b128 v[104:107], v37\n"
      "v_mfma_f32_16x16x32_bf16 v[18:21], a[16:19], v[112:115], v[18:21]\n"
      "v_mfma_f32_16x16x32_bf16 v[22:25], a[20:23], v[112:115], v[22:25]\n"
      "ds_read_b128 v[108:111], v37 offset:1024\n"
      "v_mfma_f32_16x16x32_bf16 v[48:51], a[16:19], v[116:119], v[48:51]\n"
      "buffer_load_dwordx4 a[84:87], v255, s[32:35], s52 offen offset:1024 sc1\n"
      "v_mfma_f32_16x16x32_bf16 v[52:55], a[20:23], v[116:119], v[52:55]\n"
      "s_waitcnt lgkmcnt(4)\n"
      "ds_read_b128 v[112:115], v36 offset:16384\n"
      "v_mfma_f32_16x16x32_bf16 v[56:59], a[16:19], v[120:123], v[56:59]\n"
      "v_mfma_f32_16x16x32_bf16 v[60:63], a[20:23], v[120:123], v[60:63]\n"
      "ds_read_b128 v[116:119], v36 offset:17408\n"
      "v_mfma_f32_16x16x32_bf16 v[72:75], a[8:11], v[124:127], v[72:75]\n"
      "v_mfma_f32_16x16x32_bf16 v[76:79], a[12:15], v[124:127], v[76:79]\n"
      "s_waitcnt vmcnt(15) lgkmcnt(4)\n"
      "ds_read_b128 v[120:123], v30 offset:57344\n"
      "v_mfma_f32_16x16x32_bf16 v[10:13], a[24:27], v[96:99], v[10:13]\n"
      "buffer_store_dwordx4 v[26:29], v255, s[48:51], s11 offen offset:1024 sc1\n"
      "v_mfma_f32_16x16x32_bf16 v[14:17], a[28:31], v[96:99], v[14:17]\n"
      "ds_read_b128 v[124:127], v30 offset:58368\n"
      "v_mfma_f32_16x16x32_bf16 v[18:21], a[24:27], v[100:103], v[18:21]\n"
      "v_mfma_f32_16x16x32_bf16 v[22:25], a[28:31], v[100:103], v[22:25]\n"
      "s_waitcnt lgkmcnt(4)\n"
      "ds_read_b128 v[96:99], v34 offset:8192\n"
      "v_mfma_f32_16x16x32_bf16 v[48:51], a[24:27], v[104:107], v[48:51]\n"
      "buffer_load_dwordx4 a[88:91], v255, s[32:35], s52 offen offset:2048 sc1\n"
      "v_mfma_f32_16x16x32_bf16 v[52:55], a[28:31], v[104:107], v[52:55]\n"
      "ds_read_b128 v[100:103], v34 offset:9216\n"
      "v_mfma_f32_16x16x32_bf16 v[56:59], a[24:27], v[108:111], v[56:59]\n"
      "v_mfma_f32_16x16x32_bf16 v[60:63], a[28:31], v[108:111], v[60:63]\n"
      "s_waitcnt lgkmcnt(4)\n"
      "ds_read_b128 v[104:107], v37 offset:16384\n"
      "v_mfma_f32_16x16x32_bf16 v[64:67], a[16:19], v[112:115], v[64:67]\n"
      "buffer_load_dwordx4 a[92:95], v255, s[32:35], s52 offen offset:3072 sc1\n"
      "v_mfma_f32_16x16x32_bf16 v[68:71], a[20:23], v[112:115], v[68:71]\n"
      "s_add_u32 s52, s52, 0x1000\n"
      "ds_read_b128 v[108:111], v31 offset:57344\n"
      "v_mfma_f32_16x16x32_bf16 v[72:75], a[16:19], v[116:119], v[72:75]\n"
      "v_mfma_f32_16x16x32_bf16 v[76:79], a[20:23], v[116:119], v[76:79]\n"
      "s_waitcnt vmcnt(16) lgkmcnt(4)\n"
      "ds_read_b128 v[112:115], v31 offset:58368\n"
      "v_mfma_f32_16x16x32_bf16 v[10:13], a[32:35], v[120:123], v[10:13]\n"
      "buffer_load_dwordx4 a[96:99], v255, s[32:35], s52 offen sc1\n"
      "v_mfma_f32_16x16x32_bf16 v[14:17], a[36:39], v[120:123], v[14:17]\n"
      "ds_read_b128 v[116:119], v35 offset:8192\n"
      "v_mfma_f32_16x16x32_bf16 v[18:21], a[32:35], v[124:127], v[18:21]\n"
      "v_mfma_f32_16x16x32_bf16 v[22:25], a[36:39], v[124:127], v[22:25]\n"
      "s_waitcnt lgkmcnt(4)\n"
      "ds_read_b128 v[120:123], v35 offset:9216\n"
      "v_mfma_f32_16x16x32_bf16 v[48:51], a[32:35], v[96:99], v[48:51]\n"
      "v_mfma_f32_16x16x32_bf16 v[52:55], a[36:39], v[96:99], v[52:55]\n"
      "ds_read_b128 v[124:127], v37 offset:17408\n"
      "v_mfma_f32_16x16x32_bf16 v[56:59], a[32:35], v[100:103], v[56:59]\n"
      "buffer_load_dwordx4 a[100:103], v255, s[32:35], s52 offen offset:1024 sc1\n"
      "v_mfma_f32_16x16x32_bf16 v[60:63], a[36:39], v[100:103], v[60:63]\n"
      "s_waitcnt vmcnt(16) lgkmcnt(4)\n"
      "ds_read_b128 v[96:99], v34 offset:57344\n"
      "v_mfma_f32_16x16x32_bf16 v[64:67], a[24:27], v[104:107], v[64:67]\n"
      "v_mfma_f32_16x16x32_bf16 v[68:71], a[28:31], v[104:107], v[68:71]\n"
      "ds_read_b128 v[100:103], v32 offset:57344\n"
      "v_mfma_f32_16x16x32_bf16 v[10:13], a[40:43], v[108:111], v[10:13]\n"
      "buffer_load_dwordx4 a[104:107], v255, s[32:35], s52 offen offset:2048 sc1\n"
      "v_mfma_f32_16x16x32_bf16 v[14:17], a[44:47], v[108:111], v[14:17]\n"
      "s_waitcnt lgkmcnt(4)\n"
      "ds_read_b128 v[104:107], v32 offset:58368\n"
      "v_mfma_f32_16x16x32_bf16 v[18:21], a[40:43], v[112:115], v[18:21]\n"
      "v_mfma_f32_16x16x32_bf16 v[22:25], a[44:47], v[112:115], v[22:25]\n"
      "ds_read_b128 v[108:111], v36 offset:8192\n"
      "v_mfma_f32_16x16x32_bf16 v[48:51], a[40:43], v[116:119], v[48:51]\n"
      "buffer_store_dwordx4 v[26:29], v255, s[48:51], s46 offen sc1\n"
      "v_mfma_f32_16x16x32_bf16 v[52:55], a[44:47], v[116:119], v[52:55]\n"
      "s_waitcnt lgkmcnt(4)\n"
      "ds_read_b128 v[112:115], v36 offset:9216\n"
      "v_mfma_f32_16x16x32_bf16 v[56:59], a[40:43], v[120:123], v[56:59]\n"
      "v_mfma_f32_16x16x32_bf16 v[60:63], a[44:47], v[120:123], v[60:63]\n"
      "ds_read_b128 v[116:119], v34 offset:58368\n"
      "v_mfma_f32_16x16x32_bf16 v[72:75], a[24:27], v[124:127], v[72:75]\n"
      "buffer_load_dwordx4 a[108:111], v255, s[32:35], s52 offen offset:3072 sc1\n"
      "v_mfma_f32_16x16x32_bf16 v[76:79], a[28:31], v[124:127], v[76:79]\n"
      "s_waitcnt vmcnt(17) lgkmcnt(4)\n"
      "ds_read_b128 v[120:123], v33 offset:57344\n"
      "s_add_u32 s52, s52, 0x1000\n"
      "v_mfma_f32_16x16x32_bf16 v[64:67], a[32:35], v[96:99], v[64:67]\n"
      "v_mfma_f32_16x16x32_bf16 v[68:71], a[36:39], v[96:99], v[68:71]\n"
      "ds_read_b128 v[124:127], v33 offset:58368\n"
      "v_mfma_f32_16x16x32_bf16 v[10:13], a[48:51], v[100:103], v[10:13]\n"
      "buffer_load_dwordx4 a[112:115], v255, s[32:35], s52 offen sc1\n"
      "v_mfma_f32_16x16x32_bf16 v[14:17], a[52:55], v[100:103], v[14:17]\n"
      "s_waitcnt lgkmcnt(4)\n"
      "ds_read_b128 v[96:99], v37 offset:8192\n"
      "v_mfma_f32_16x16x32_bf16 v[18:21], a[48:51], v[104:107], v[18:21]\n"
      "v_mfma_f32_16x16x32_bf16 v[22:25], a[52:55], v[104:107], v[22:25]\n"
      "ds_read_b128 v[100:103], v37 offset:9216\n"
      "v_mfma_f32_16x16x32_bf16 v[48:51], a[48:51], v[108:111], v[48:51]\n"
      "v_mfma_f32_16x16x32_bf16 v[52:55], a[52:55], v[108:111], v[52:55]\n"
      "s_waitcnt lgkmcnt(4)\n"
      "ds_read_b128 v[104:107], v35 offset:57344\n"
      "v_mfma_f32_16x16x32_bf16 v[56:59], a[48:51], v[112:115], v[56:59]\n"
      "buffer_load_dwordx4 a[116:119], v255, s[32:35], s52 offen offset:1024 sc1\n"
      "v_mfma_f32_16x16x32_bf16 v[60:63], a[52:55], v[112:115], v[60:63]\n"
      "ds_read_b128 v[108:111], v35 offset:58368\n"
      "v_mfma_f32_16x16x32_bf16 v[72:75], a[32:35], v[116:119], v[72:75]\n"
      "v_mfma_f32_16x16x32_bf16 v[76:79], a[36:39], v[116:119], v[76:79]\n"
      "s_waitcnt vmcnt(17) lgkmcnt(4)\n"
      "ds_read_b128 v[112:115], v34 offset:24576\n"
      "v_mfma_f32_16x16x32_bf16 v[10:13], a[56:59], v[120:123], v[10:13]\n"
      "buffer_load_dwordx4 a[120:123], v255, s[32:35], s52 offen offset:2048 sc1\n"
      "v_mfma_f32_16x16x32_bf16 v[14:17], a[60:63], v[120:123], v[14:17]\n"
      "ds_read_b128 v[116:119], v34 offset:25600\n"
      "v_mfma_f32_16x16x32_bf16 v[18:21], a[56:59], v[124:127], v[18:21]\n"
      "v_mfma_f32_16x16x32_bf16 v[22:25], a[60:63], v[124:127], v[22:25]\n"
      "s_waitcnt lgkmcnt(4)\n"
      "ds_read_b128 v[120:123], v34 offset:40960\n"
      "v_mfma_f32_16x16x32_bf16 v[48:51], a[56:59], v[96:99], v[48:51]\n"
      "buffer_load_dwordx4 a[124:127], v255, s[32:35], s52 offen offset:3072 sc1\n"
      "v_mfma_f32_16x16x32_bf16 v[52:55], a[60:63], v[96:99], v[52:55]\n"
      "s_add_u32 m0, s69, 0xc000\n"
      "ds_read_b128 v[124:127], v34 offset:41984\n"
      "s_add_u32 s74, s43, 0x300\n"
      "v_mfma_f32_16x16x32_bf16 v[56:59], a[56:59], v[100:103], v[56:59]\n"
      "v_mfma_f32_16x16x32_bf16 v[60:63], a[60:63], v[100:103], v[60:63]\n"
      "s_waitcnt lgkmcnt(4)\n"
      "ds_read_b128 v[96:99], v36 offset:57344\n"
      "v_mfma_f32_16x16x32_bf16 v[64:67], a[40:43], v[104:107], v[64:67]\n"
      "buffer_store_dwordx4 v[26:29], v255, s[48:51], s46 offen offset:1024 sc1\n"
      "v_mfma_f32_16x16x32_bf16 v[68:71], a[44:47], v[104:107], v[68:71]\n"
      "ds_read_b128 v[100:103], v35 offset:24576\n"
      "v_mfma_f32_16x16x32_bf16 v[72:75], a[40:43], v[108:111], v[72:75]\n"
      "v_mfma_f32_16x16x32_bf16 v[76:79], a[44:47], v[108:111], v[76:79]\n"
      "s_waitcnt vmcnt(18) lgkmcnt(4)\n"
      "ds_read_b128 v[104:107], v35 offset:25600\n"
      "v_mfma_f32_16x16x32_bf16 v[10:13], a[64:67], v[112:115], v[10:13]\n"
      "s_barrier\n"
      "v_mfma_f32_16x16x32_bf16 v[14:17], a[68:71], v[112:115], v[14:17]\n"
      "ds_read_b128 v[108:111], v35 offset:40960\n"
      "v_mfma_f32_16x16x32_bf16 v[18:21], a[64:67], v[116:119], v[18:21]\n"
      "buffer_load_dwordx4 v4, s[12:15], s74 offen lds\n"
      "v_mfma_f32_16x16x32_bf16 v[22:25], a[68:71], v[116:119], v[22:25]\n"
      "s_waitcnt lgkmcnt(4)\n"
      "ds_read_b128 v[112:115], v35 offset:41984\n"
      "v_mfma_f32_16x16x32_bf16 v[48:51], a[64:67], v[120:123], v[48:51]\n"
      "v_mfma_f32_16x16x32_bf16 v[52:55], a[68:71], v[120:123], v[52:55]\n"
      "ds_read_b128 v[116:119], v36 offset:58368\n"
      "v_mfma_f32_16x16x32_bf16 v[56:59], a[64:67], v[124:127], v[56:59]\n"
      "buffer_load_dwordx4 v5, s[12:15], s74 offen offset:1024 lds\n"
      "v_mfma_f32_16x16x32_bf16 v[60:63], a[68:71], v[124:127], v[60:63]\n"
      "s_waitcnt vmcnt(17) lgkmcnt(4)\n"
      "ds_read_b128 v[120:123], v37 offset:57344\n"
      "s_add_u32 m0, s69, 0xe000\n"
      "v_mfma_f32_16x16x32_bf16 v[64:67], a[48:51], v[96:99], v[64:67]\n"
      "s_add_u32 s74, s43, 0x400\n"
      "v_mfma_f32_16x16x32_bf16 v[68:71], a[52:55], v[96:99], v[68:71]\n"
      "ds_read_b128 v[124:127], v36 offset:24576\n"
      "v_mfma_f32_16x16x32_bf16 v[10:13], a[72:75], v[100:103], v[10:13]\n"
      "buffer_load_dwordx4 v4, s[12:15], s74 offen lds\n"
      "v_mfma_f32_16x16x32_bf16 v[14:17], a[76:79], v[100:103], v[14:17]\n"
      "s_waitcnt lgkmcnt(4)\n"
      "ds_read_b128 v[96:99], v36 offset:25600\n"
      "v_mfma_f32_16x16x32_bf16 v[18:21], a[72:75], v[104:107], v[18:21]\n"
      "v_mfma_f32_16x16x32_bf16 v[22:25], a[76:79], v[104:107], v[22:25]\n"
      "ds_read_b128 v[100:103], v36 offset:40960\n"
      "v_mfma_f32_16x16x32_bf16 v[48:51], a[72:75], v[108:111], v[48:51]\n"
      "buffer_load_dwordx4 v5, s[12:15], s74 offen offset:1024 lds\n"
      "v_mfma_f32_16x16x32_bf16 v[52:55], a[76:79], v[108:111], v[52:55]\n"
      "s_waitcnt lgkmcnt(4)\n"
      "ds_read_b128 v[104:107], v36 offset:41984\n"
      "s_add_u32 s52, s52, 0x1000\n"
      "v_mfma_f32_16x16x32_bf16 v[56:59], a[72:75], v[112:115], v[56:59]\n"
      "v_mfma_f32_16x16x32_bf16 v[60:63], a[76:79], v[112:115], v[60:63]\n"
      "ds_read_b128 v[108:111], v37 offset:58368\n"
      "v_mfma_f32_16x16x32_bf16 v[72:75], a[48:51], v[116:119], v[72:75]\n"
      "buffer_load_dwordx4 a[128:131], v255, s[32:35], s52 offen sc1\n"
      "v_mfma_f32_16x16x32_bf16 v[76:79], a[52:55], v[116:119], v[76:79]\n"
      "s_waitcnt vmcnt(18) lgkmcnt(4)\n"
      "ds_read_b128 v[112:115], v37 offset:24576\n"
      "v_mfma_f32_16x16x32_bf16 v[64:67], a[56:59], v[120:123], v[64:67]\n"
      "v_mfma_f32_16x16x32_bf16 v[68:71], a[60:63], v[120:123], v[68:71]\n"
      "ds_read_b128 v[116:119], v37 offset:25600\n"
      "v_mfma_f32_16x16x32_bf16 v[10:13], a[80:83], v[124:127], v[10:13]\n"
      "buffer_store_dwordx4 v[26:29], v255, s[48:51], s47 offen sc1\n"
      "v_mfma_f32_16x16x32_bf16 v[14:17], a[84:87], v[124:127], v[14:17]\n"
      "s_waitcnt lgkmcnt(4)\n"
      "ds_read_b128 v[120:123], v37 offset:40960\n"
      "v_mfma_f32_16x16x32_bf16 v[18:21], a[80:83], v[96:99], v[18:21]\n"
      "v_mfma_f32_16x16x32_bf16 v[22:25], a[84:87], v[96:99], v[22:25]\n"
      "ds_read_b128 v[124:127], v37 offset:41984\n"
      "v_mfma_f32_16x16x32_bf16 v[48:51], a[80:83], v[100:103], v[48:51]\n"
      "v_mfma_f32_16x16x32_bf16 v[52:55], a[84:87], v[100:103], v[52:55]\n"
      "s_waitcnt lgkmcnt(4)\n"
      "ds_read_b128 v[96:99], v30 offset:40960\n"
      "v_mfma_f32_16x16x32_bf16 v[56:59], a[80:83], v[104:107], v[56:59]\n"
      "buffer_load_dwordx4 a[132:135], v255, s[32:35], s52 offen offset:1024 sc1\n"
      "v_mfma_f32_16x16x32_bf16 v[60:63], a[84:87], v[104:107], v[60:63]\n"
      "ds_read_b128 v[100:103], v30 offset:41984\n"
      "v_mfma_f32_16x16x32_bf16 v[72:75], a[56:59], v[108:111], v[72:75]\n"
      "v_mfma_f32_16x16x32_bf16 v[76:79], a[60:63], v[108:111], v[76:79]\n"
      "s_waitcnt vmcnt(17) lgkmcnt(4)\n"
      "ds_read_b128 v[104:107], v34 offset:32768\n"
      "v_mfma_f32_16x16x32_bf16 v[10:13], a[88:91], v[112:115], v[10:13]\n"
      "buffer_load_dwordx4 a[136:139], v255, s[32:35], s52 offen offset:2048 sc1\n"
      "v_mfma_f32_16x16x32_bf16 v[14:17], a[92:95], v[112:115], v[14:17]\n"
      "ds_read_b128 v[108:111], v34 offset:33792\n"
      "v_mfma_f32_16x16x32_bf16 v[18:21], a[88:91], v[116:119], v[18:21]\n"
      "v_mfma_f32_16x16x32_bf16 v[22:25], a[92:95], v[116:119], v[22:25]\n"
      "s_waitcnt lgkmcnt(4)\n"
      "ds_read_b128 v[112:115], v34 offset:49152\n"
      "v_mfma_f32_16x16x32_bf16 v[48:51], a[88:91], v[120:123], v[48:51]\n"
      "buffer_load_dwordx4 a[140:143], v255, s[32:35], s52 offen offset:3072 sc1\n"
      "v_mfma_f32_16x16x32_bf16 v[52:55], a[92:95], v[120:123], v[52:55]\n"
      "s_add_u32 s52, s52, 0x1000\n"
      "ds_read_b128 v[116:119], v34 offset:50176\n"
      "v_mfma_f32_16x16x32_bf16 v[56:59], a[88:91], v[124:127], v[56:59]\n"
      "v_mfma_f32_16x16x32_bf16 v[60:63], a[92:95], v[124:127], v[60:63]\n"
      "s_waitcnt lgkmcnt(4)\n"
      "ds_read_b128 v[120:123], v31 offset:40960\n"
      "v_mfma_f32_16x16x32_bf16 v[64:67], a[64:67], v[96:99], v[64:67]\n"
      "buffer_load_dwordx4 a[144:147], v255, s[32:35], s52 offen sc1\n"
      "v_mfma_f32_16x16x32_bf16 v[68:71], a[68:71], v[96:99], v[68:71]\n"
      "ds_read_b128 v[124:127], v35 offset:32768\n"
      "v_mfma_f32_16x16x32_bf16 v[72:75], a[64:67], v[100:103], v[72:75]\n"
      "v_mfma_f32_16x16x32_bf16 v[76:79], a[68:71], v[100:103], v[76:79]\n"
      "s_waitcnt vmcnt(18) lgkmcnt(4)\n"
      "ds_read_b128 v[96:99], v35 offset:33792\n"
      "v_mfma_f32_16x16x32_bf16 v[10:13], a[96:99], v[104:107], v[10:13]\n"
      "v_mfma_f32_16x16x32_bf16 v[14:17], a[100:103], v[104:107], v[14:17]\n"
      "ds_read_b128 v[100:103], v35 offset:49152\n"
      "v_mfma_f32_16x16x32_bf16 v[18:21], a[96:99], v[108:111], v[18:21]\n"
      "buffer_load_dwordx4 a[148:151], v255, s[32:35], s52 offen offset:1024 sc1\n"
      "v_mfma_f32_16x16x32_bf16 v[22:25], a[100:103], v[108:111], v[22:25]\n"
      "s_waitcnt lgkmcnt(4)\n"
      "ds_read_b128 v[104:107], v35 offset:50176\n"
      "v_mfma_f32_16x16x32_bf16 v[48:51], a[96:99], v[112:115], v[48:51]\n"
      "v_mfma_f32_16x16x32_bf16 v[52:55], a[100:103], v[112:115], v[52:55]\n"
      "ds_read_b128 v[108:111], v31 offset:41984\n"
      "v_mfma_f32_16x16x32_bf16 v[56:59], a[96:99], v[116:119], v[56:59]\n"
      "buffer_store_dwordx4 v[26:29], v255, s[48:51], s47 offen offset:1024 sc1\n"
      "v_mfma_f32_16x16x32_bf16 v[60:63], a[100:103], v[116:119], v[60:63]\n"
      "s_waitcnt vmcnt(17) lgkmcnt(4)\n"
      "ds_read_b128 v[112:115], v32 offset:40960\n"
      "v_mfma_f32_16x16x32_bf16 v[64:67], a[72:75], v[120:123], v[64:67]\n"
      "v_mfma_f32_16x16x32_bf16 v[68:71], a[76:79], v[120:123], v[68:71]\n"
      "ds_read_b128 v[116:119], v36 offset:32768\n"
      "v_mfma_f32_16x16x32_bf16 v[10:13], a[104:107], v[124:127], v[10:13]\n"
      "buffer_load_dwordx4 a[152:155], v255, s[32:35], s52 offen offset:2048 sc1\n"
      "v_mfma_f32_16x16x32_bf16 v[14:17], a[108:111], v[124:127], v[14:17]\n"
      "s_waitcnt lgkmcnt(4)\n"
      "ds_read_b128 v[120:123], v36 offset:33792\n"
      "v_mfma_f32_16x16x32_bf16 v[18:21], a[104:107], v[96:99], v[18:21]\n"
      "v_mfma_f32_16x16x32_bf16 v[22:25], a[108:111], v[96:99], v[22:25]\n"
      "ds_read_b128 v[124:127], v36 offset:49152\n"
      "v_mfma_f32_16x16x32_bf16 v[48:51], a[104:107], v[100:103], v[48:51]\n"
      "buffer_load_dwordx4 a[156:159], v255, s[32:35], s52 offen offset:3072 sc1\n"
      "v_mfma_f32_16x16x32_bf16 v[52:55], a[108:111], v[100:103], v[52:55]\n"
      "s_waitcnt lgkmcnt(4)\n"
      "ds_read_b128 v[96:99], v36 offset:50176\n"
      "s_add_u32 s52, s52, 0x1000\n"
      "v_mfma_f32_16x16x32_bf16 v[56:59], a[104:107], v[104:107], v[56:59]\n"
      "v_mfma_f32_16x16x32_bf16 v[60:63], a[108:111], v[104:107], v[60:63]\n"
      "ds_read_b128 v[100:103], v32 offset:41984\n"
      "v_mfma_f32_16x16x32_bf16 v[72:75], a[72:75], v[108:111], v[72:75]\n"
      "buffer_load_dwordx4 a[160:163], v255, s[32:35], s52 offen sc1\n"
      "v_mfma_f32_16x16x32_bf16 v[76:79], a[76:79], v[108:111], v[76:79]\n"
      "s_waitcnt vmcnt(18) lgkmcnt(4)\n"
      "ds_read_b128 v[104:107], v37 offset:32768\n"
      "v_mfma_f32_16x16x32_bf16 v[64:67], a[80:83], v[112:115], v[64:67]\n"
      "v_mfma_f32_16x16x32_bf16 v[68:71], a[84:87], v[112:115], v[68:71]\n"
      "ds_read_b128 v[108:111], v37 offset:33792\n"
      "v_mfma_f32_16x16x32_bf16 v[10:13], a[112:115], v[116:119], v[10:13]\n"
      "v_mfma_f32_16x16x32_bf16 v[14:17], a[116:119], v[116:119], v[14:17]\n"
      "s_waitcnt lgkmcnt(4)\n"
      "ds_read_b128 v[112:115], v37 offset:49152\n"
      "v_mfma_f32_16x16x32_bf16 v[18:21], a[112:115], v[120:123], v[18:21]\n"
      "buffer_load_dwordx4 a[164:167], v255, s[32:35], s52 offen offset:1024 sc1\n"
      "v_mfma_f32_16x16x32_bf16 v[22:25], a[116:119], v[120:123], v[22:25]\n"
      "ds_read_b128 v[116:119], v37 offset:50176\n"
      "v_mfma_f32_16x16x32_bf16 v[48:51], a[112:115], v[124:127], v[48:51]\n"
      "v_mfma_f32_16x16x32_bf16 v[52:55], a[116:119], v[124:127], v[52:55]\n"
      "s_waitcnt lgkmcnt(4)\n"
      "ds_read_b128 v[120:123], v33 offset:40960\n"
      "v_mfma_f32_16x16x32_bf16 v[56:59], a[112:115], v[96:99], v[56:59]\n"
      "buffer_load_dwordx4 a[168:171], v255, s[32:35], s52 offen offset:2048 sc1\n"
      "v_mfma_f32_16x16x32_bf16 v[60:63], a[116:119], v[96:99], v[60:63]\n"
      "ds_read_b128 v[124:127], v33 offset:41984\n"
      "v_mfma_f32_16x16x32_bf16 v[72:75], a[80:83], v[100:103], v[72:75]\n"
      "v_mfma_f32_16x16x32_bf16 v[76:79], a[84:87], v[100:103], v[76:79]\n"
      "s_waitcnt vmcnt(18) lgkmcnt(4)\n"
      "ds_read_b128 v[96:99], v38 offset:16384\n"
      "v_mfma_f32_16x16x32_bf16 v[10:13], a[120:123], v[104:107], v[10:13]\n"
      "buffer_store_dwordx4 v[26:29], v255, s[48:51], s77 offen sc1\n"
      "v_mfma_f32_16x16x32_bf16 v[14:17], a[124:127], v[104:107], v[14:17]\n"
      "ds_read_b128 v[100:103], v38 offset:17408\n"
      "v_mfma_f32_16x16x32_bf16 v[18:21], a[120:123], v[108:111], v[18:21]\n"
      "v_mfma_f32_16x16x32_bf16 v[22:25], a[124:127], v[108:111], v[22:25]\n"
      "s_waitcnt lgkmcnt(4)\n"
      "ds_read_b128 v[104:107], v38 offset:8192\n"
      "v_mfma_f32_16x16x32_bf16 v[48:51], a[120:123], v[112:115], v[48:51]\n"
      "buffer_load_dwordx4 a[172:175], v255, s[32:35], s52 offen offset:3072 sc1\n"
      "v_mfma_f32_16x16x32_bf16 v[52:55], a[124:127], v[112:115], v[52:55]\n"
      "s_add_u32 m0, s69, 0x10000\n"
      "ds_read_b128 v[108:111], v38 offset:9216\n"
      "s_add_u32 s74, s43, 0x500\n"
      "v_mfma_f32_16x16x32_bf16 v[56:59], a[120:123], v[116:119], v[56:59]\n"
      "s_waitcnt vmcnt(15)\n"
      "s_barrier\n"
      "v_mfma_f32_16x16x32_bf16 v[60:63], a[124:127], v[116:119], v[60:63]\n"
      "s_waitcnt lgkmcnt(4)\n"
      "ds_read_b128 v[112:115], v30 offset:49152\n"
      "v_mfma_f32_16x16x32_bf16 v[64:67], a[88:91], v[120:123], v[64:67]\n"
      "v_mfma_f32_16x16x32_bf16 v[68:71], a[92:95], v[120:123], v[68:71]\n"
      "ds_read_b128 v[116:119], v39 offset:16384\n"
      "v_mfma_f32_16x16x32_bf16 v[72:75], a[88:91], v[124:127], v[72:75]\n"
      "buffer_load_dwordx4 v4, s[12:15], s74 offen lds\n"
      "v_mfma_f32_16x16x32_bf16 v[76:79], a[92:95], v[124:127], v[76:79]\n"
      "s_waitcnt vmcnt(13) lgkmcnt(4)\n"
      "ds_read_b128 v[120:123], v39 offset:17408\n"
      "v_mfma_f32_16x16x32_bf16 v[10:13], a[128:131], v[96:99], v[10:13]\n"
      "v_mfma_f32_16x16x32_bf16 v[14:17], a[132:135], v[96:99], v[14:17]\n"
      "ds_read_b128 v[124:127], v39 offset:8192\n"
      "v_mfma_f32_16x16x32_bf16 v[18:21], a[128:131], v[100:103], v[18:21]\n"
      "buffer_load_dwordx4 v5, s[12:15], s74 offen offset:1024 lds\n"
      "v_mfma_f32_16x16x32_bf16 v[22:25], a[132:135], v[100:103], v[22:25]\n"
      "s_waitcnt lgkmcnt(4)\n"
      "ds_read_b128 v[96:99], v39 offset:9216\n"
      "s_add_u32 m0, s69, 0x12000\n"
      "v_mfma_f32_16x16x32_bf16 v[48:51], a[128:131], v[104:107], v[48:51]\n"
      "s_add_u32 s74, s43, 0x600\n"
      "v_mfma_f32_16x16x32_bf16 v[52:55], a[132:135], v[104:107], v[52:55]\n"
      "ds_read_b128 v[100:103], v30 offset:50176\n"
      "v_mfma_f32_16x16x32_bf16 v[56:59], a[128:131], v[108:111], v[56:59]\n"
      "buffer_load_dwordx4 v4, s[12:15], s74 offen lds\n"
      "v_mfma_f32_16x16x32_bf16 v[60:63], a[132:135], v[108:111], v[60:63]\n"
      "s_waitcnt vmcnt(13) lgkmcnt(4)\n"
      "ds_read_b128 v[104:107], v31 offset:49152\n"
      "v_mfma_f32_16x16x32_bf16 v[64:67], a[96:99], v[112:115], v[64:67]\n"
      "v_mfma_f32_16x16x32_bf16 v[68:71], a[100:103], v[112:115], v[68:71]\n"
      "ds_read_b128 v[108:111], v40 offset:16384\n"
      "v_mfma_f32_16x16x32_bf16 v[10:13], a[136:139], v[116:119], v[10:13]\n"
      "buffer_load_dwordx4 v5, s[12:15], s74 offen offset:1024 lds\n"
      "v_mfma_f32_16x16x32_bf16 v[14:17], a[140:143], v[116:119], v[14:17]\n"
      "s_waitcnt lgkmcnt(4)\n"
      "ds_read_b128 v[112:115], v40 offset:17408\n"
      "s_add_u32 m0, s69, 0x14000\n"
      "v_mfma_f32_16x16x32_bf16 v[18:21], a[136:139], v[120:123], v[18:21]\n"
      "s_add_u32 s74, s43, 0x700\n"
      "v_mfma_f32_16x16x32_bf16 v[22:25], a[140:143], v[120:123], v[22:25]\n"
      "ds_read_b128 v[116:119], v40 offset:8192\n"
      "v_mfma_f32_16x16x32_bf16 v[48:51], a[136:139], v[124:127], v[48:51]\n"
      "buffer_store_dwordx4 v[26:29], v233, s[48:51], s77 offen sc1\n"
      "v_mfma_f32_16x16x32_bf16 v[52:55], a[140:143], v[124:127], v[52:55]\n"
      "s_waitcnt lgkmcnt(4)\n"
      "ds_read_b128 v[120:123], v40 offset:9216\n"
      "v_mfma_f32_16x16x32_bf16 v[56:59], a[136:139], v[96:99], v[56:59]\n"
      "v_mfma_f32_16x16x32_bf16 v[60:63], a[140:143], v[96:99], v[60:63]\n"
      "ds_read_b128 v[124:127], v31 offset:50176\n"
      "v_mfma_f32_16x16x32_bf16 v[72:75], a[96:99], v[100:103], v[72:75]\n"
      "buffer_load_dwordx4 v4, s[12:15], s74 offen lds\n"
      "v_mfma_f32_16x16x32_bf16 v[76:79], a[100:103], v[100:103], v[76:79]\n"
      "s_waitcnt vmcnt(14) lgkmcnt(4)\n"
      "ds_read_b128 v[96:99], v41 offset:16384\n"
      "v_mfma_f32_16x16x32_bf16 v[64:67], a[104:107], v[104:107], v[64:67]\n"
      "v_mfma_f32_16x16x32_bf16 v[68:71], a[108:111], v[104:107], v[68:71]\n"
      "ds_read_b128 v[100:103], v41 offset:17408\n"
      "v_mfma_f32_16x16x32_bf16 v[10:13], a[144:147], v[108:111], v[10:13]\n"
      "v_mfma_f32_16x16x32_bf16 v[14:17], a[148:151], v[108:111], v[14:17]\n"
      "s_waitcnt lgkmcnt(4)\n"
      "ds_read_b128 v[104:107], v41 offset:8192\n"
      "v_mfma_f32_16x16x32_bf16 v[18:21], a[144:147], v[112:115], v[18:21]\n"
      "buffer_load_dwordx4 v5, s[12:15], s74 offen offset:1024 lds\n"
      "v_mfma_f32_16x16x32_bf16 v[22:25], a[148:151], v[112:115], v[22:25]\n"
      "s_add_u32 s52, s52, 0x1000\n"
      "ds_read_b128 v[108:111], v41 offset:9216\n"
      "v_mfma_f32_16x16x32_bf16 v[48:51], a[144:147], v[116:119], v[48:51]\n"
      "v_mfma_f32_16x16x32_bf16 v[52:55], a[148:151], v[116:119], v[52:55]\n"
      "s_waitcnt lgkmcnt(4)\n"
      "ds_read_b128 v[112:115], v32 offset:49152\n"
      "v_mfma_f32_16x16x32_bf16 v[56:59], a[144:147], v[120:123], v[56:59]\n"
      "buffer_load_dwordx4 a[176:179], v255, s[32:35], s52 offen sc1\n"
      "v_mfma_f32_16x16x32_bf16 v[60:63], a[148:151], v[120:123], v[60:63]\n"
      "ds_read_b128 v[116:119], v32 offset:50176\n"
      "v_mfma_f32_16x16x32_bf16 v[72:75], a[104:107], v[124:127], v[72:75]\n"
      "v_mfma_f32_16x16x32_bf16 v[76:79], a[108:111], v[124:127], v[76:79]\n"
      "s_waitcnt vmcnt(13) lgkmcnt(4)\n"
      "ds_read_b128 v[120:123], v38\n"
      "v_mfma_f32_16x16x32_bf16 v[10:13], a[152:155], v[96:99], v[10:13]\n"
      "buffer_load_dwordx4 a[180:183], v255, s[32:35], s52 offen offset:1024 sc1\n"
      "v_mfma_f32_16x16x32_bf16 v[14:17], a[156:159], v[96:99], v[14:17]\n"
      "ds_read_b128 v[124:127], v38 offset:1024\n"
      "v_mfma_f32_16x16x32_bf16 v[18:21], a[152:155], v[100:103], v[18:21]\n"
      "v_mfma_f32_16x16x32_bf16 v[22:25], a[156:159], v[100:103], v[22:25]\n"
      "s_waitcnt lgkmcnt(4)\n"
      "ds_read_b128 v[96:99], v30 offset:16384\n"
      "v_mfma_f32_16x16x32_bf16 v[48:51], a[152:155], v[104:107], v[48:51]\n"
      "buffer_load_dwordx4 a[184:187], v255, s[32:35], s52 offen offset:2048 sc1\n"
      "v_mfma_f32_16x16x32_bf16 v[52:55], a[156:159], v[104:107], v[52:55]\n"
      "ds_read_b128 v[100:103], v30 offset:17408\n"
      "v_mfma_f32_16x16x32_bf16 v[56:59], a[152:155], v[108:111], v[56:59]\n"
      "v_mfma_f32_16x16x32_bf16 v[60:63], a[156:159], v[108:111], v[60:63]\n"
      "s_waitcnt lgkmcnt(4)\n"
      "ds_read_b128 v[104:107], v33 offset:49152\n"
      "v_mfma_f32_16x16x32_bf16 v[64:67], a[112:115], v[112:115], v[64:67]\n"
      "buffer_store_dwordx4 v[26:29], v255, s[48:51], s78 offen sc1\n"
      "v_mfma_f32_16x16x32_bf16 v[68:71], a[116:119], v[112:115], v[68:71]\n"
      "ds_read_b128 v[108:111], v39\n"
      "v_mfma_f32_16x16x32_bf16 v[72:75], a[112:115], v[116:119], v[72:75]\n"
      "v_mfma_f32_16x16x32_bf16 v[76:79], a[116:119], v[116:119], v[76:79]\n"
      "s_waitcnt vmcnt(14) lgkmcnt(4)\n"
      "ds_read_b128 v[112:115], v39 offset:1024\n"
      "v_mfma_f32_16x16x32_bf16 v[10:13], a[160:163], v[120:123], v[10:13]\n"
      "v_mfma_f32_16x16x32_bf16 v[14:17], a[164:167], v[120:123], v[14:17]\n"
      "ds_read_b128 v[116:119], v31 offset:16384\n"
      "v_mfma_f32_16x16x32_bf16 v[18:21], a[160:163], v[124:127], v[18:21]\n"
      "buffer_load_dwordx4 a[188:191], v255, s[32:35], s52 offen offset:3072 sc1\n"
      "v_mfma_f32_16x16x32_bf16 v[22:25], a[164:167], v[124:127], v[22:25]\n"
      "s_waitcnt lgkmcnt(4)\n"
      "ds_read_b128 v[120:123], v31 offset:17408\n"
      "s_add_u32 s52, s52, 0x1000\n"
      "v_mfma_f32_16x16x32_bf16 v[48:51], a[160:163], v[96:99], v[48:51]\n"
      "v_mfma_f32_16x16x32_bf16 v[52:55], a[164:167], v[96:99], v[52:55]\n"
      "ds_read_b128 v[124:127], v33 offset:50176\n"
      "v_mfma_f32_16x16x32_bf16 v[56:59], a[160:163], v[100:103], v[56:59]\n"
      "buffer_load_dwordx4 a[192:195], v255, s[32:35], s52 offen sc1\n"
      "v_mfma_f32_16x16x32_bf16 v[60:63], a[164:167], v[100:103], v[60:63]\n"
      "s_waitcnt vmcnt(13) lgkmcnt(4)\n"
      "ds_read_b128 v[96:99], v30 offset:57344\n"
      "v_mfma_f32_16x16x32_bf16 v[64:67], a[120:123], v[104:107], v[64:67]\n"
      "v_mfma_f32_16x16x32_bf16 v[68:71], a[124:127], v[104:107], v[68:71]\n"
      "ds_read_b128 v[100:103], v40\n"
      "v_mfma_f32_16x16x32_bf16 v[10:13], a[168:171], v[108:111], v[10:13]\n"
      "buffer_load_dwordx4 a[196:199], v255, s[32:35], s52 offen offset:1024 sc1\n"
      "v_mfma_f32_16x16x32_bf16 v[14:17], a[172:175], v[108:111], v[14:17]\n"
      "s_waitcnt lgkmcnt(4)\n"
      "ds_read_b128 v[104:107], v40 offset:1024\n"
      "v_mfma_f32_16x16x32_bf16 v[18:21], a[168:171], v[112:115], v[18:21]\n"
      "v_mfma_f32_16x16x32_bf16 v[22:25], a[172:175], v[112:115], v[22:25]\n"
      "ds_read_b128 v[108:111], v32 offset:16384\n"
      "v_mfma_f32_16x16x32_bf16 v[48:51], a[168:171], v[116:119], v[48:51]\n"
      "buffer_load_dwordx4 a[200:203], v255, s[32:35], s52 offen offset:2048 sc1\n"
      "v_mfma_f32_16x16x32_bf16 v[52:55], a[172:175], v[116:119], v[52:55]\n"
      "s_waitcnt lgkmcnt(4)\n"
      "ds_read_b128 v[112:115], v32 offset:17408\n"
      "v_mfma_f32_16x16x32_bf16 v[56:59], a[168:171], v[120:123], v[56:59]\n"
      "v_mfma_f32_16x16x32_bf16 v[60:63], a[172:175], v[120:123], v[60:63]\n"
      "ds_read_b128 v[116:119], v30 offset:58368\n"
      "v_mfma_f32_16x16x32_bf16 v[72:75], a[120:123], v[124:127], v[72:75]\n"
      "v_mfma_f32_16x16x32_bf16 v[76:79], a[124:127], v[124:127], v[76:79]\n"
      "s_waitcnt vmcnt(6) lgkmcnt(4)\n"
      "ds_read_b128 v[120:123], v41\n"
      "v_mfma_f32_16x16x32_bf16 v[64:67], a[128:131], v[96:99], v[64:67]\n"
      "buffer_store_dwordx4 v[26:29], v233, s[48:51], s78 offen sc1\n"
      "v_mfma_f32_16x16x32_bf16 v[68:71], a[132:135], v[96:99], v[68:71]\n"
      "ds_read_b128 v[124:127], v41 offset:1024\n"
      "v_mfma_f32_16x16x32_bf16 v[10:13], a[176:179], v[100:103], v[10:13]\n"
      "v_mfma_f32_16x16x32_bf16 v[14:17], a[180:183], v[100:103], v[14:17]\n"
      "s_waitcnt lgkmcnt(4)\n"
      "ds_read_b128 v[96:99], v33 offset:16384\n"
      "v_mfma_f32_16x16x32_bf16 v[18:21], a[176:179], v[104:107], v[18:21]\n"
      "buffer_load_dwordx4 a[204:207], v255, s[32:35], s52 offen offset:3072 sc1\n"
      "v_mfma_f32_16x16x32_bf16 v[22:25], a[180:183], v[104:107], v[22:25]\n"
      "s_add_u32 s52, s52, 0x1000\n"
      "ds_read_b128 v[100:103], v33 offset:17408\n"
      "v_mfma_f32_16x16x32_bf16 v[48:51], a[176:179], v[108:111], v[48:51]\n"
      "v_mfma_f32_16x16x32_bf16 v[52:55], a[180:183], v[108:111], v[52:55]\n"
      "s_waitcnt lgkmcnt(4)\n"
      "ds_read_b128 v[104:107], v31 offset:57344\n"
      "v_mfma_f32_16x16x32_bf16 v[56:59], a[176:179], v[112:115], v[56:59]\n"
      "buffer_load_dwordx4 a[208:211], v255, s[32:35], s52 offen sc1\n"
      "v_mfma_f32_16x16x32_bf16 v[60:63], a[180:183], v[112:115], v[60:63]\n"
      "ds_read_b128 v[108:111], v31 offset:58368\n"
      "v_mfma_f32_16x16x32_bf16 v[72:75], a[128:131], v[116:119], v[72:75]\n"
      "v_mfma_f32_16x16x32_bf16 v[76:79], a[132:135], v[116:119], v[76:79]\n"
      "s_waitcnt vmcnt(6) lgkmcnt(4)\n"
      "ds_read_b128 v[112:115], v30\n"
      "v_mfma_f32_16x16x32_bf16 v[10:13], a[184:187], v[120:123], v[10:13]\n"
      "buffer_load_dwordx4 a[212:215], v255, s[32:35], s52 offen offset:1024 sc1\n"
      "v_mfma_f32_16x16x32_bf16 v[14:17], a[188:191], v[120:123], v[14:17]\n"
      "ds_read_b128 v[116:119], v30 offset:1024\n"
      "v_mfma_f32_16x16x32_bf16 v[18:21], a[184:187], v[124:127], v[18:21]\n"
      "v_mfma_f32_16x16x32_bf16 v[22:25], a[188:191], v[124:127], v[22:25]\n"
      "s_waitcnt lgkmcnt(4)\n"
      "ds_read_b128 v[120:123], v30 offset:24576\n"
      "v_mfma_f32_16x16x32_bf16 v[48:51], a[184:187], v[96:99], v[48:51]\n"
      "v_mfma_f32_16x16x32_bf16 v[52:55], a[188:191], v[96:99], v[52:55]\n"
      "ds_read_b128 v[124:127], v30 offset:25600\n"
      "v_mfma_f32_16x16x32_bf16 v[56:59], a[184:187], v[100:103], v[56:59]\n"
      "buffer_load_dwordx4 a[216:219], v255, s[32:35], s52 offen offset:2048 sc1\n"
      "v_mfma_f32_16x16x32_bf16 v[60:63], a[188:191], v[100:103], v[60:63]\n"
      "s_waitcnt lgkmcnt(4)\n"
      "ds_read_b128 v[96:99], v32 offset:57344\n"
      "v_mfma_f32_16x16x32_bf16 v[64:67], a[136:139], v[104:107], v[64:67]\n"
      "v_mfma_f32_16x16x32_bf16 v[68:71], a[140:143], v[104:107], v[68:71]\n"
      "ds_read_b128 v[100:103], v31\n"
      "v_mfma_f32_16x16x32_bf16 v[72:75], a[136:139], v[108:111], v[72:75]\n"
      "buffer_load_dwordx4 a[220:223], v255, s[32:35], s52 offen offset:3072 sc1\n"
      "v_mfma_f32_16x16x32_bf16 v[76:79], a[140:143], v[108:111], v[76:79]\n"
      "s_waitcnt vmcnt(7) lgkmcnt(4)\n"
      "ds_read_b128 v[104:107], v31 offset:1024\n"
      "s_add_u32 s52, s52, 0x1000\n"
      "v_mfma_f32_16x16x32_bf16 v[10:13], a[192:195], v[112:115], v[10:13]\n"
      "s_barrier\n"
      "s_add_u32 s70, s9, 0x108000\n"
      "buffer_store_dwordx2 v[246:247], v245, s[48:51], s70 offen sc1\n"
      "v_mfma_f32_16x16x32_bf16 v[14:17], a[196:199], v[112:115], v[14:17]\n"
      "ds_read_b128 v[108:111], v31 offset:24576\n"
      "v_mfma_f32_16x16x32_bf16 v[18:21], a[192:195], v[116:119], v[18:21]\n"
      "buffer_store_dwordx4 v[26:29], v234, s[48:51], s79 offen sc1\n"
      "v_mfma_f32_16x16x32_bf16 v[22:25], a[196:199], v[116:119], v[22:25]\n"
      "s_waitcnt lgkmcnt(4)\n"
      "ds_read_b128 v[112:115], v31 offset:25600\n"
      "v_mfma_f32_16x16x32_bf16 v[48:51], a[192:195], v[120:123], v[48:51]\n"
      "v_mfma_f32_16x16x32_bf16 v[52:55], a[196:199], v[120:123], v[52:55]\n"
      "ds_read_b128 v[116:119], v32 offset:58368\n"
      "v_mfma_f32_16x16x32_bf16 v[56:59], a[192:195], v[124:127], v[56:59]\n"
      "buffer_load_dwordx4 a[224:227], v255, s[32:35], s52 offen sc1\n"
      "v_mfma_f32_16x16x32_bf16 v[60:63], a[196:199], v[124:127], v[60:63]\n"
      "s_waitcnt vmcnt(7) lgkmcnt(4)\n"
      "ds_read_b128 v[120:123], v33 offset:57344\n"
      "v_mfma_f32_16x16x32_bf16 v[64:67], a[144:147], v[96:99], v[64:67]\n"
      "v_mfma_f32_16x16x32_bf16 v[68:71], a[148:151], v[96:99], v[68:71]\n"
      "ds_read_b128 v[124:127], v32\n"
      "v_mfma_f32_16x16x32_bf16 v[10:13], a[200:203], v[100:103], v[10:13]\n"
      "buffer_load_dwordx4 a[228:231], v255, s[32:35], s52 offen offset:1024 sc1\n"
      "v_mfma_f32_16x16x32_bf16 v[14:17], a[204:207], v[100:103], v[14:17]\n"
      "s_waitcnt lgkmcnt(4)\n"
      "ds_read_b128 v[96:99], v32 offset:1024\n"
      "v_mfma_f32_16x16x32_bf16 v[18:21], a[200:203], v[104:107], v[18:21]\n"
      "v_mfma_f32_16x16x32_bf16 v[22:25], a[204:207], v[104:107], v[22:25]\n"
      "ds_read_b128 v[100:103], v32 offset:24576\n"
      "v_mfma_f32_16x16x32_bf16 v[48:51], a[200:203], v[108:111], v[48:51]\n"
      "v_mfma_f32_16x16x32_bf16 v[52:55], a[204:207], v[108:111], v[52:55]\n"
      "s_waitcnt lgkmcnt(4)\n"
      "ds_read_b128 v[104:107], v32 offset:25600\n"
      "v_mfma_f32_16x16x32_bf16 v[56:59], a[200:203], v[112:115], v[56:59]\n"
      "buffer_load_dwordx4 a[232:235], v255, s[32:35], s52 offen offset:2048 sc1\n"
      "v_mfma_f32_16x16x32_bf16 v[60:63], a[204:207], v[112:115], v[60:63]\n"
      "ds_read_b128 v[108:111], v33 offset:58368\n"
      "v_mfma_f32_16x16x32_bf16 v[72:75], a[144:147], v[116:119], v[72:75]\n"
      "v_mfma_f32_16x16x32_bf16 v[76:79], a[148:151], v[116:119], v[76:79]\n"
      "s_waitcnt vmcnt(7) lgkmcnt(4)\n"
      "ds_read_b128 v[112:115], v33\n"
      "v_mfma_f32_16x16x32_bf16 v[64:67], a[152:155], v[120:123], v[64:67]\n"
      "buffer_load_dwordx4 a[236:239], v255, s[32:35], s52 offen offset:3072 sc1\n"
      "v_mfma_f32_16x16x32_bf16 v[68:71], a[156:159], v[120:123], v[68:71]\n"
      "s_add_u32 s52, s52, 0x1000\n"
      "ds_read_b128 v[116:119], v33 offset:1024\n"
      "v_mfma_f32_16x16x32_bf16 v[10:13], a[208:211], v[124:127], v[10:13]\n"
      "v_mfma_f32_16x16x32_bf16 v[14:17], a[212:215], v[124:127], v[14:17]\n"
      "s_waitcnt lgkmcnt(4)\n"
      "ds_read_b128 v[120:123], v33 offset:24576\n"
      "v_mfma_f32_16x16x32_bf16 v[18:21], a[208:211], v[96:99], v[18:21]\n"
      "buffer_load_dwordx4 a[240:243], v255, s[32:35], s52 offen sc1\n"
      "v_mfma_f32_16x16x32_bf16 v[22:25], a[212:215], v[96:99], v[22:25]\n"
      "ds_read_b128 v[124:127], v33 offset:25600\n"
      "v_mfma_f32_16x16x32_bf16 v[48:51], a[208:211], v[100:103], v[48:51]\n"
      "s_barrier\n"
      "v_mfma_f32_16x16x32_bf16 v[52:55], a[212:215], v[100:103], v[52:55]\n"
      "s_waitcnt lgkmcnt(4)\n"
      "ds_read_b128 v[96:99], v34\n"
      "v_mfma_f32_16x16x32_bf16 v[56:59], a[208:211], v[104:107], v[56:59]\n"
      "buffer_store_dwordx4 v[26:29], v235, s[48:51], s79 offen sc1\n"
      "v_mfma_f32_16x16x32_bf16 v[60:63], a[212:215], v[104:107], v[60:63]\n"
      "ds_read_b128 v[100:103], v34 offset:1024\n"
      "v_mfma_f32_16x16x32_bf16 v[72:75], a[152:155], v[108:111], v[72:75]\n"
      "v_mfma_f32_16x16x32_bf16 v[76:79], a[156:159], v[108:111], v[76:79]\n"
      "s_waitcnt vmcnt(8) lgkmcnt(4)\n"
      "ds_read_b128 v[104:107], v30 offset:8192\n"
      "v_mfma_f32_16x16x32_bf16 v[10:13], a[216:219], v[112:115], v[10:13]\n"
      "v_mfma_f32_16x16x32_bf16 v[14:17], a[220:223], v[112:115], v[14:17]\n"
      "ds_read_b128 v[108:111], v30 offset:9216\n"
      "v_mfma_f32_16x16x32_bf16 v[18:21], a[216:219], v[116:119], v[18:21]\n"
      "buffer_load_dwordx4 a[244:247], v255, s[32:35], s52 offen offset:1024 sc1\n"
      "v_mfma_f32_16x16x32_bf16 v[22:25], a[220:223], v[116:119], v[22:25]\n"
      "s_waitcnt lgkmcnt(4)\n"
      "ds_read_b128 v[112:115], v30 offset:32768\n"
      "v_mfma_f32_16x16x32_bf16 v[48:51], a[216:219], v[120:123], v[48:51]\n"
      "v_mfma_f32_16x16x32_bf16 v[52:55], a[220:223], v[120:123], v[52:55]\n"
      "ds_read_b128 v[116:119], v30 offset:33792\n"
      "v_mfma_f32_16x16x32_bf16 v[56:59], a[216:219], v[124:127], v[56:59]\n"
      "buffer_load_dword v251, v250, s[36:39], 0 offen\n"
      "v_mfma_f32_16x16x32_bf16 v[60:63], a[220:223], v[124:127], v[60:63]\n"
      "s_waitcnt lgkmcnt(4)\n"
      "ds_read_b128 v[120:123], v35\n"
      "v_mfma_f32_16x16x32_bf16 v[64:67], a[160:163], v[96:99], v[64:67]\n"
      "buffer_load_dwordx4 a[248:251], v255, s[32:35], s52 offen offset:2048 sc1\n"
      "v_mfma_f32_16x16x32_bf16 v[68:71], a[164:167], v[96:99], v[68:71]\n"
      "ds_read_b128 v[124:127], v31 offset:8192\n"
      "v_mfma_f32_16x16x32_bf16 v[72:75], a[160:163], v[100:103], v[72:75]\n"
      "v_mfma_f32_16x16x32_bf16 v[76:79], a[164:167], v[100:103], v[76:79]\n"
      "s_waitcnt vmcnt(7) lgkmcnt(4)\n"
      "ds_read_b128 v[96:99], v31 offset:9216\n"
      "v_mfma_f32_16x16x32_bf16 v[10:13], a[224:227], v[104:107], v[10:13]\n"
      "buffer_load_dwordx4 a[252:255], v255, s[32:35], s52 offen offset:3072 sc1\n"
      "v_mfma_f32_16x16x32_bf16 v[14:17], a[228:231], v[104:107], v[14:17]\n"
      "s_add_u32 m0, s69, 0x4000\n"
      "ds_read_b128 v[100:103], v31 offset:32768\n"
      "v_mfma_f32_16x16x32_bf16 v[18:21], a[224:227], v[108:111], v[18:21]\n"
      "v_mfma_f32_16x16x32_bf16 v[22:25], a[228:231], v[108:111], v[22:25]\n"
      "s_waitcnt lgkmcnt(4)\n"
      "ds_read_b128 v[104:107], v31 offset:33792\n"
      "v_mfma_f32_16x16x32_bf16 v[48:51], a[224:227], v[112:115], v[48:51]\n"
      "buffer_load_dwordx4 v2, s[4:7], s55 offen lds\n"
      "v_mfma_f32_16x16x32_bf16 v[52:55], a[228:231], v[112:115], v[52:55]\n"
      "ds_read_b128 v[108:111], v35 offset:1024\n"
      "v_mfma_f32_16x16x32_bf16 v[56:59], a[224:227], v[116:119], v[56:59]\n"
      "v_mfma_f32_16x16x32_bf16 v[60:63], a[228:231], v[116:119], v[60:63]\n"
      "s_waitcnt vmcnt(7) lgkmcnt(4)\n"
      "ds_read_b128 v[112:115], v36\n"
      "v_mfma_f32_16x16x32_bf16 v[64:67], a[168:171], v[120:123], v[64:67]\n"
      "v_mfma_f32_16x16x32_bf16 v[68:71], a[172:175], v[120:123], v[68:71]\n"
      "ds_read_b128 v[116:119], v32 offset:8192\n"
      "v_mfma_f32_16x16x32_bf16 v[10:13], a[232:235], v[124:127], v[10:13]\n"
      "buffer_load_dwordx4 v3, s[4:7], s55 offen offset:1024 lds\n"
      "v_mfma_f32_16x16x32_bf16 v[14:17], a[236:239], v[124:127], v[14:17]\n"
      "s_waitcnt lgkmcnt(4)\n"
      "ds_read_b128 v[120:123], v32 offset:9216\n"
      "s_add_u32 m0, s69, 0x16000\n"
      "v_mfma_f32_16x16x32_bf16 v[18:21], a[232:235], v[96:99], v[18:21]\n"
      "v_mfma_f32_16x16x32_bf16 v[22:25], a[236:239], v[96:99], v[22:25]\n"
      "ds_read_b128 v[124:127], v32 offset:32768\n"
      "v_mfma_f32_16x16x32_bf16 v[48:51], a[232:235], v[100:103], v[48:51]\n"
      "buffer_load_dwordx4 v4, s[12:15], s44 offen lds\n"
      "v_mfma_f32_16x16x32_bf16 v[52:55], a[236:239], v[100:103], v[52:55]\n"
      "s_waitcnt lgkmcnt(4)\n"
      "ds_read_b128 v[96:99], v32 offset:33792\n"
      "v_mfma_f32_16x16x32_bf16 v[56:59], a[232:235], v[104:107], v[56:59]\n"
      "v_mfma_f32_16x16x32_bf16 v[60:63], a[236:239], v[104:107], v[60:63]\n"
      "ds_read_b128 v[100:103], v36 offset:1024\n"
      "v_mfma_f32_16x16x32_bf16 v[72:75], a[168:171], v[108:111], v[72:75]\n"
      "buffer_load_dwordx4 v5, s[12:15], s44 offen offset:1024 lds\n"
      "v_mfma_f32_16x16x32_bf16 v[76:79], a[172:175], v[108:111], v[76:79]\n"
      "s_waitcnt vmcnt(7) lgkmcnt(4)\n"
      "ds_read_b128 v[104:107], v33 offset:8192\n"
      "s_add_u32 m0, s69, 0x18000\n"
      "v_mfma_f32_16x16x32_bf16 v[64:67], a[176:179], v[112:115], v[64:67]\n"
      "s_add_u32 s74, s44, 0x100\n"
      "v_mfma_f32_16x16x32_bf16 v[68:71], a[180:183], v[112:115], v[68:71]\n"
      "ds_read_b128 v[108:111], v33 offset:9216\n"
      "v_mfma_f32_16x16x32_bf16 v[10:13], a[240:243], v[116:119], v[10:13]\n"
      "buffer_load_dwordx4 v4, s[12:15], s74 offen lds\n"
      "v_mfma_f32_16x16x32_bf16 v[14:17], a[244:247], v[116:119], v[14:17]\n"
      "s_waitcnt lgkmcnt(4)\n"
      "ds_read_b128 v[112:115], v33 offset:32768\n"
      "v_mfma_f32_16x16x32_bf16 v[18:21], a[240:243], v[120:123], v[18:21]\n"
      "v_mfma_f32_16x16x32_bf16 v[22:25], a[244:247], v[120:123], v[22:25]\n"
      "ds_read_b128 v[116:119], v33 offset:33792\n"
      "v_mfma_f32_16x16x32_bf16 v[48:51], a[240:243], v[124:127], v[48:51]\n"
      "buffer_load_dwordx4 v5, s[12:15], s74 offen offset:1024 lds\n"
      "v_mfma_f32_16x16x32_bf16 v[52:55], a[244:247], v[124:127], v[52:55]\n"
      "s_waitcnt lgkmcnt(4)\n"
      "ds_read_b128 v[120:123], v37\n"
      "s_add_u32 m0, s69, 0x1a000\n"
      "v_mfma_f32_16x16x32_bf16 v[56:59], a[240:243], v[96:99], v[56:59]\n"
      "s_add_u32 s74, s44, 0x200\n"
      "v_mfma_f32_16x16x32_bf16 v[60:63], a[244:247], v[96:99], v[60:63]\n"
      "ds_read_b128 v[124:127], v37 offset:1024\n"
      "v_mfma_f32_16x16x32_bf16 v[72:75], a[176:179], v[100:103], v[72:75]\n"
      "buffer_load_dwordx4 v4, s[12:15], s74 offen lds\n"
      "v_mfma_f32_16x16x32_bf16 v[76:79], a[180:183], v[100:103], v[76:79]\n"
      "s_waitcnt vmcnt(7) lgkmcnt(4)\n"
      "ds_read_b128 v[96:99], v34 offset:8192\n"
      "v_mfma_f32_16x16x32_bf16 v[10:13], a[248:251], v[104:107], v[10:13]\n"
      "v_mfma_f32_16x16x32_bf16 v[14:17], a[252:255], v[104:107], v[14:17]\n"
      "ds_read_b128 v[100:103], v34 offset:9216\n"
      "v_mfma_f32_16x16x32_bf16 v[18:21], a[248:251], v[108:111], v[18:21]\n"
      "v_mfma_f32_16x16x32_bf16 v[22:25], a[252:255], v[108:111], v[22:25]\n"
      "s_waitcnt lgkmcnt(4)\n"
      "ds_read_b128 v[104:107], v35 offset:8192\n"
      "v_mfma_f32_16x16x32_bf16 v[48:51], a[248:251], v[112:115], v[48:51]\n"
      "s_mov_b32 s72, 0x108000\n"
      "buffer_load_dwordx2 v[242:243], v241, s[48:51], s72 offen sc1\n"
      "v_mfma_f32_16x16x32_bf16 v[52:55], a[252:255], v[112:115], v[52:55]\n"
      "ds_read_b128 v[108:111], v35 offset:9216\n"
      "v_mfma_f32_16x16x32_bf16 v[56:59], a[248:251], v[116:119], v[56:59]\n"
      "buffer_load_dwordx4 v5, s[12:15], s74 offen offset:1024 lds\n"
      "v_mfma_f32_16x16x32_bf16 v[60:63], a[252:255], v[116:119], v[60:63]\n"
      "s_waitcnt lgkmcnt(4)\n"
      "ds_read_b128 v[112:115], v36 offset:8192\n"
      "s_add_u32 m0, s69, 0x1c000\n"
      "v_mfma_f32_16x16x32_bf16 v[64:67], a[184:187], v[120:123], v[64:67]\n"
      "s_add_u32 s74, s44, 0x300\n"
      "v_cvt_pk_bf16_f32 v160, v10, v11\n"
      "v_cvt_pk_bf16_f32 v161, v12, v13\n"
      "v_mfma_f32_16x16x32_bf16 v[68:71], a[188:191], v[120:123], v[68:71]\n"
      "ds_read_b128 v[116:119], v36 offset:9216\n"
      "v_cvt_pk_bf16_f32 v162, v14, v15\n"
      "v_mfma_f32_16x16x32_bf16 v[72:75], a[184:187], v[124:127], v[72:75]\n"
      "buffer_load_dwordx4 v4, s[12:15], s74 offen lds\n"
      "v_mfma_f32_16x16x32_bf16 v[76:79], a[188:191], v[124:127], v[76:79]\n"
      "s_waitcnt lgkmcnt(4)\n"
      "ds_read_b128 v[120:123], v37 offset:8192\n"
      "v_cvt_pk_bf16_f32 v163, v16, v17\n"
      "v_mfma_f32_16x16x32_bf16 v[64:67], a[192:195], v[96:99], v[64:67]\n"
      "v_min_u32 v160, -2, v160\n"
      "v_min_u32 v161, -2, v161\n"
      "v_mfma_f32_16x16x32_bf16 v[68:71], a[196:199], v[96:99], v[68:71]\n"
      "ds_read_b128 v[124:127], v37 offset:9216\n"
      "v_min_u32 v162, -2, v162\n"
      "v_mfma_f32_16x16x32_bf16 v[72:75], a[192:195], v[100:103], v[72:75]\n"
      "s_waitcnt vmcnt(6)\n"
      "s_barrier\n"
      "ds_read_b128 v[128:131], v9 offset:16384\n"
      "v_mfma_f32_16x16x32_bf16 v[76:79], a[196:199], v[100:103], v[76:79]\n"
      "s_waitcnt lgkmcnt(5)\n"
      "ds_read_b128 v[96:99], v34 offset:16384\n"
      "ds_read_b128 v[132:135], v9 offset:17408\n"
      "v_min_u32 v163, -2, v163\n"
      "v_mfma_f32_16x16x32_bf16 v[64:67], a[200:203], v[104:107], v[64:67]\n"
      "buffer_load_dwordx4 v5, s[12:15], s74 offen offset:1024 lds\n"
      "v_mfma_f32_16x16x32_bf16 v[68:71], a[204:207], v[104:107], v[68:71]\n"
      "s_add_u32 m0, s69, 0x1e000\n"
      "ds_read_b128 v[100:103], v34 offset:17408\n"
      "s_add_u32 s74, s44, 0x400\n"
      "v_cvt_pk_bf16_f32 v164, v18, v19\n"
      "v_cvt_pk_bf16_f32 v165, v20, v21\n"
      "v_mfma_f32_16x16x32_bf16 v[72:75], a[200:203], v[108:111], v[72:75]\n"
      "buffer_store_dwordx4 v[160:163], v255, s[48:51], s11 offen sc1\n"
      "v_cvt_pk_bf16_f32 v166, v22, v23\n"
      "v_mfma_f32_16x16x32_bf16 v[76:79], a[204:207], v[108:111], v[76:79]\n"
      "s_waitcnt lgkmcnt(6)\n"
      "ds_read_b128 v[104:107], v35 offset:16384\n"
      "v_cvt_pk_bf16_f32 v167, v24, v25\n"
      "v_min_u32 v164, -2, v164\n"
      "v_mfma_f32_16x16x32_bf16 v[64:67], a[208:211], v[112:115], v[64:67]\n"
      "v_min_u32 v165, -2, v165\n"
      "v_min_u32 v166, -2, v166\n"
      "v_mfma_f32_16x16x32_bf16 v[68:71], a[212:215], v[112:115], v[68:71]\n"
      "ds_read_b128 v[108:111], v35 offset:17408\n"
      "v_min_u32 v167, -2, v167\n"
      "v_mfma_f32_16x16x32_bf16 v[72:75], a[208:211], v[116:119], v[72:75]\n"
      "buffer_load_dwordx4 v4, s[12:15], s74 offen lds\n"
      "v_mfma_f32_16x16x32_bf16 v[76:79], a[212:215], v[116:119], v[76:79]\n"
      "s_waitcnt lgkmcnt(6)\n"
      "ds_read_b128 v[112:115], v36 offset:16384\n"
      "v_cvt_pk_bf16_f32 v168, v48, v49\n"
      "v_cvt_pk_bf16_f32 v169, v50, v51\n"
      "v_mfma_f32_16x16x32_bf16 v[64:67], a[216:219], v[120:123], v[64:67]\n"
      "v_cvt_pk_bf16_f32 v170, v52, v53\n"
      "v_cvt_pk_bf16_f32 v171, v54, v55\n"
      "v_mfma_f32_16x16x32_bf16 v[68:71], a[220:223], v[120:123], v[68:71]\n"
      "ds_read_b128 v[116:119], v36 offset:17408\n"
      "v_min_u32 v168, -2, v168\n"
      "v_mfma_f32_16x16x32_bf16 v[72:75], a[216:219], v[124:127], v[72:75]\n"
      "buffer_store_dwordx4 v[164:167], v255, s[48:51], s11 offen offset:1024 sc1\n"
      "v_min_u32 v169, -2, v169\n"
      "v_mfma_f32_16x16x32_bf16 v[76:79], a[220:223], v[124:127], v[76:79]\n"
      "s_waitcnt lgkmcnt(4)\n"
      "ds_read_b128 v[120:123], v37 offset:16384\n"
      "v_min_u32 v170, -2, v170\n"
      "v_min_u32 v171, -2, v171\n"
      "v_mfma_f32_16x16x32_bf16 v[64:67], a[224:227], v[96:99], v[64:67]\n"
      "v_cvt_pk_bf16_f32 v172, v56, v57\n"
      "v_cvt_pk_bf16_f32 v173, v58, v59\n"
      "v_mfma_f32_16x16x32_bf16 v[68:71], a[228:231], v[96:99], v[68:71]\n"
      "ds_read_b128 v[124:127], v37 offset:17408\n"
      "v_cvt_pk_bf16_f32 v174, v60, v61\n"
      "v_lshlrev_b32 v80, 16, v128\n"
      "v_mfma_f32_16x16x32_bf16 v[72:75], a[224:227], v[100:103], v[72:75]\n"
      "v_cvt_pk_bf16_f32 v175, v62, v63\n"
      "v_min_u32 v172, -2, v172\n"
      "v_and_b32 v81, 0xffff0000, v128\n"
      "v_mfma_f32_16x16x32_bf16 v[76:79], a[228:231], v[100:103], v[76:79]\n"
      "s_waitcnt lgkmcnt(4)\n"
      "ds_read_b128 v[96:99], v34 offset:24576\n"
      "v_min_u32 v173, -2, v173\n"
      "v_lshlrev_b32 v82, 16, v129\n"
      "v_and_b32 v83, 0xffff0000, v129\n"
      "v_mfma_f32_16x16x32_bf16 v[64:67], a[232:235], v[104:107], v[64:67]\n"
      "buffer_store_dwordx4 v[168:171], v255, s[48:51], s46 offen sc1\n"
      "v_lshlrev_b32 v84, 16, v130\n"
      "v_mfma_f32_16x16x32_bf16 v[68:71], a[236:239], v[104:107], v[68:71]\n"
      "ds_read_b128 v[100:103], v34 offset:25600\n"
      "v_min_u32 v174, -2, v174\n"
      "v_min_u32 v175, -2, v175\n"
      "v_and_b32 v85, 0xffff0000, v130\n"
      "v_lshlrev_b32 v86, 16, v131\n"
      "v_mfma_f32_16x16x32_bf16 v[72:75], a[232:235], v[108:111], v[72:75]\n"
      "v_and_b32 v87, 0xffff0000, v131\n"
      "v_lshlrev_b32 v88, 16, v132\n"
      "v_mfma_f32_16x16x32_bf16 v[76:79], a[236:239], v[108:111], v[76:79]\n"
      "s_waitcnt lgkmcnt(4)\n"
      "ds_read_b128 v[104:107], v35 offset:24576\n"
      "v_and_b32 v89, 0xffff0000, v132\n"
      "v_mfma_f32_16x16x32_bf16 v[64:67], a[240:243], v[112:115], v[64:67]\n"
      "buffer_store_dwordx4 v[172:175], v255, s[48:51], s46 offen offset:1024 sc1\n"
      "v_lshlrev_b32 v90, 16, v133\n"
      "v_mfma_f32_16x16x32_bf16 v[68:71], a[244:247], v[112:115], v[68:71]\n"
      "ds_read_b128 v[108:111], v35 offset:25600\n"
      "v_and_b32 v91, 0xffff0000, v133\n"
      "v_lshlrev_b32 v92, 16, v134\n"
      "v_mfma_f32_16x16x32_bf16 v[72:75], a[240:243], v[116:119], v[72:75]\n"
      "v_and_b32 v93, 0xffff0000, v134\n"
      "v_lshlrev_b32 v94, 16, v135\n"
      "v_mfma_f32_16x16x32_bf16 v[76:79], a[244:247], v[116:119], v[76:79]\n"
      "s_waitcnt lgkmcnt(4)\n"
      "ds_read_b128 v[112:115], v36 offset:24576\n"
      "v_and_b32 v95, 0xffff0000, v135\n"
      "v_mfma_f32_16x16x32_bf16 v[64:67], a[248:251], v[120:123], v[64:67]\n"
      "buffer_load_dwordx4 v5, s[12:15], s74 offen offset:1024 lds\n"
      "v_mfma_f32_16x16x32_bf16 v[68:71], a[252:255], v[120:123], v[68:71]\n"
      "s_add_u32 m0, s69, 0x20000\n"
      "ds_read_b128 v[116:119], v36 offset:25600\n"
      "s_add_u32 s74, s44, 0x500\n"
      "v_mfma_f32_16x16x32_bf16 v[72:75], a[248:251], v[124:127], v[72:75]\n"
      "v_mfma_f32_16x16x32_bf16 v[76:79], a[252:255], v[124:127], v[76:79]\n"
      "s_waitcnt lgkmcnt(4)\n"
      "ds_read_b128 v[120:123], v37 offset:24576\n"
      "v_mfma_f32_16x16x32_bf16 v[80:83], a[0:3], v[96:99], v[80:83]\n"
      "buffer_load_dwordx4 v4, s[12:15], s74 offen lds\n"
      "v_mfma_f32_16x16x32_bf16 v[84:87], a[4:7], v[96:99], v[84:87]\n"
      "ds_read_b128 v[124:127], v37 offset:25600\n"
      "v_cvt_pk_bf16_f32 v176, v64, v65\n"
      "v_cvt_pk_bf16_f32 v177, v66, v67\n"
      "v_mfma_f32_16x16x32_bf16 v[88:91], a[0:3], v[100:103], v[88:91]\n"
      "s_waitcnt vmcnt(9)\n"
      "s_barrier\n"
      "v_cvt_pk_bf16_f32 v178, v68, v69\n"
      "v_mfma_f32_16x16x32_bf16 v[92:95], a[4:7], v[100:103], v[92:95]\n"
      "s_waitcnt lgkmcnt(4)\n"
      "ds_read_b128 v[96:99], v34 offset:32768\n"
      "v_cvt_pk_bf16_f32 v179, v70, v71\n"
      "v_min_u32 v176, -2, v176\n"
      "v_mfma_f32_16x16x32_bf16 v[80:83], a[8:11], v[104:107], v[80:83]\n"
      "buffer_load_dwordx4 v5, s[12:15], s74 offen offset:1024 lds\n"
      "v_min_u32 v177, -2, v177\n"
      "v_mfma_f32_16x16x32_bf16 v[84:87], a[12:15], v[104:107], v[84:87]\n"
      "s_add_u32 m0, s69, 0x22000\n"
      "ds_read_b128 v[100:103], v34 offset:33792\n"
      "s_add_u32 s74, s44, 0x600\n"
      "v_min_u32 v178, -2, v178\n"
      "v_min_u32 v179, -2, v179\n"
      "v_mfma_f32_16x16x32_bf16 v[88:91], a[8:11], v[108:111], v[88:91]\n"
      "v_cvt_pk_bf16_f32 v180, v72, v73\n"
      "v_cvt_pk_bf16_f32 v181, v74, v75\n"
      "v_mfma_f32_16x16x32_bf16 v[92:95], a[12:15], v[108:111], v[92:95]\n"
      "s_waitcnt lgkmcnt(4)\n"
      "ds_read_b128 v[104:107], v35 offset:32768\n"
      "v_cvt_pk_bf16_f32 v182, v76, v77\n"
      "v_mfma_f32_16x16x32_bf16 v[80:83], a[16:19], v[112:115], v[80:83]\n"
      "buffer_store_dwordx4 v[176:179], v255, s[48:51], s47 offen sc1\n"
      "v_mfma_f32_16x16x32_bf16 v[84:87], a[20:23], v[112:115], v[84:87]\n"
      "ds_read_b128 v[108:111], v35 offset:33792\n"
      "v_cvt_pk_bf16_f32 v183, v78, v79\n"
      "v_min_u32 v180, -2, v180\n"
      "v_mfma_f32_16x16x32_bf16 v[88:91], a[16:19], v[116:119], v[88:91]\n"
      "v_min_u32 v181, -2, v181\n"
      "v_min_u32 v182, -2, v182\n"
      "v_mfma_f32_16x16x32_bf16 v[92:95], a[20:23], v[116:119], v[92:95]\n"
      "s_waitcnt lgkmcnt(4)\n"
      "ds_read_b128 v[112:115], v36 offset:32768\n"
      "v_min_u32 v183, -2, v183\n"
      "v_mfma_f32_16x16x32_bf16 v[80:83], a[24:27], v[120:123], v[80:83]\n"
      "v_mfma_f32_16x16x32_bf16 v[84:87], a[28:31], v[120:123], v[84:87]\n"
      "ds_read_b128 v[116:119], v36 offset:33792\n"
      "v_mfma_f32_16x16x32_bf16 v[88:91], a[24:27], v[124:127], v[88:91]\n"
      "buffer_store_dwordx4 v[180:183], v255, s[48:51], s47 offen offset:1024 sc1\n"
      "v_mfma_f32_16x16x32_bf16 v[92:95], a[28:31], v[124:127], v[92:95]\n"
      "s_waitcnt lgkmcnt(4)\n"
      "ds_read_b128 v[120:123], v37 offset:32768\n"
      "v_mfma_f32_16x16x32_bf16 v[80:83], a[32:35], v[96:99], v[80:83]\n"
      "v_mfma_f32_16x16x32_bf16 v[84:87], a[36:39], v[96:99], v[84:87]\n"
      "ds_read_b128 v[124:127], v37 offset:33792\n"
      "v_mfma_f32_16x16x32_bf16 v[88:91], a[32:35], v[100:103], v[88:91]\n"
      "buffer_load_dwordx4 v4, s[12:15], s74 offen lds\n"
      "v_mfma_f32_16x16x32_bf16 v[92:95], a[36:39], v[100:103], v[92:95]\n"
      "s_waitcnt lgkmcnt(4)\n"
      "ds_read_b128 v[96:99], v34 offset:40960\n"
      "v_mfma_f32_16x16x32_bf16 v[80:83], a[40:43], v[104:107], v[80:83]\n"
      "v_mfma_f32_16x16x32_bf16 v[84:87], a[44:47], v[104:107], v[84:87]\n"
      "ds_read_b128 v[100:103], v34 offset:41984\n"
      "v_mfma_f32_16x16x32_bf16 v[88:91], a[40:43], v[108:111], v[88:91]\n"
      "buffer_load_dwordx4 v5, s[12:15], s74 offen offset:1024 lds\n"
      "v_mfma_f32_16x16x32_bf16 v[92:95], a[44:47], v[108:111], v[92:95]\n"
      "s_waitcnt lgkmcnt(4)\n"
      "ds_read_b128 v[104:107], v35 offset:40960\n"
      "s_add_u32 m0, s69, 0x24000\n"
      "v_mfma_f32_16x16x32_bf16 v[80:83], a[48:51], v[112:115], v[80:83]\n"
      "s_add_u32 s74, s44, 0x700\n"
      "v_mfma_f32_16x16x32_bf16 v[84:87], a[52:55], v[112:115], v[84:87]\n"
      "ds_read_b128 v[108:111], v35 offset:41984\n"
      "v_mfma_f32_16x16x32_bf16 v[88:91], a[48:51], v[116:119], v[88:91]\n"
      "buffer_load_dwordx4 v4, s[12:15], s74 offen lds\n"
      "v_mfma_f32_16x16x32_bf16 v[92:95], a[52:55], v[116:119], v[92:95]\n"
      "s_waitcnt lgkmcnt(4)\n"
      "ds_read_b128 v[112:115], v36 offset:40960\n"
      "v_mfma_f32_16x16x32_bf16 v[80:83], a[56:59], v[120:123], v[80:83]\n"
      "v_mfma_f32_16x16x32_bf16 v[84:87], a[60:63], v[120:123], v[84:87]\n"
      "ds_read_b128 v[116:119], v36 offset:41984\n"
      "v_mfma_f32_16x16x32_bf16 v[88:91], a[56:59], v[124:127], v[88:91]\n"
      "buffer_load_dwordx4 v5, s[12:15], s74 offen offset:1024 lds\n"
      "v_mfma_f32_16x16x32_bf16 v[92:95], a[60:63], v[124:127], v[92:95]\n"
      "s_waitcnt lgkmcnt(4)\n"
      "ds_read_b128 v[120:123], v37 offset:40960\n"
      "s_add_u32 m0, s59, 0\n"
      "v_mfma_f32_16x16x32_bf16 v[80:83], a[64:67], v[96:99], v[80:83]\n"
      "s_add_u32 s74, s58, 0\n"
      "v_mfma_f32_16x16x32_bf16 v[84:87], a[68:71], v[96:99], v[84:87]\n"
      "ds_read_b128 v[124:127], v37 offset:41984\n"
      "v_mfma_f32_16x16x32_bf16 v[88:91], a[64:67], v[100:103], v[88:91]\n"
      "s_waitcnt vmcnt(8)\n"
      "s_barrier\n"
      "v_mfma_f32_16x16x32_bf16 v[92:95], a[68:71], v[100:103], v[92:95]\n"
      "s_waitcnt lgkmcnt(4)\n"
      "ds_read_b128 v[96:99], v34 offset:49152\n"
      "v_mfma_f32_16x16x32_bf16 v[80:83], a[72:75], v[104:107], v[80:83]\n"
      "buffer_load_dwordx4 v236, s[16:19], s74 offen lds\n"
      "v_mfma_f32_16x16x32_bf16 v[84:87], a[76:79], v[104:107], v[84:87]\n"
      "ds_read_b128 v[100:103], v34 offset:50176\n"
      "v_mfma_f32_16x16x32_bf16 v[88:91], a[72:75], v[108:111], v[88:91]\n"
      "v_mfma_f32_16x16x32_bf16 v[92:95], a[76:79], v[108:111], v[92:95]\n"
      "s_waitcnt lgkmcnt(4)\n"
      "ds_read_b128 v[104:107], v35 offset:49152\n"
      "v_mfma_f32_16x16x32_bf16 v[80:83], a[80:83], v[112:115], v[80:83]\n"
      "v_cmp_ne_u64 vcc, s[60:61], v[242:243]\n"
      "s_nop 0\n"
      "s_cbranch_vccnz L5_%=\n"
      "L6_%=:\n"
      "v_mfma_f32_16x16x32_bf16 v[84:87], a[84:87], v[112:115], v[84:87]\n"
      "ds_read_b128 v[108:111], v35 offset:50176\n"
      "v_mfma_f32_16x16x32_bf16 v[88:91], a[80:83], v[116:119], v[88:91]\n"
      "buffer_load_dwordx4 v237, s[16:19], s74 offen offset:1024 lds\n"
      "v_mfma_f32_16x16x32_bf16 v[92:95], a[84:87], v[116:119], v[92:95]\n"
      "s_waitcnt lgkmcnt(4)\n"
      "ds_read_b128 v[112:115], v36 offset:49152\n"
      "v_mfma_f32_16x16x32_bf16 v[80:83], a[88:91], v[120:123], v[80:83]\n"
      "v_mfma_f32_16x16x32_bf16 v[84:87], a[92:95], v[120:123], v[84:87]\n"
      "ds_read_b128 v[116:119], v36 offset:50176\n"
      "v_mfma_f32_16x16x32_bf16 v[88:91], a[88:91], v[124:127], v[88:91]\n"
      "buffer_load_dwordx4 v[136:139], v255, s[48:51], s53 offen sc1\n"
      "v_mfma_f32_16x16x32_bf16 v[92:95], a[92:95], v[124:127], v[92:95]\n"
      "s_waitcnt lgkmcnt(4)\n"
      "ds_read_b128 v[120:123], v37 offset:49152\n"
      "v_mfma_f32_16x16x32_bf16 v[80:83], a[96:99], v[96:99], v[80:83]\n"
      "v_mfma_f32_16x16x32_bf16 v[84:87], a[100:103], v[96:99], v[84:87]\n"
      "ds_read_b128 v[124:127], v37 offset:50176\n"
      "v_mfma_f32_16x16x32_bf16 v[88:91], a[96:99], v[100:103], v[88:91]\n"
      "v_mfma_f32_16x16x32_bf16 v[92:95], a[100:103], v[100:103], v[92:95]\n"
      "s_waitcnt lgkmcnt(4)\n"
      "ds_read_b128 v[96:99], v34 offset:57344\n"
      "v_mfma_f32_16x16x32_bf16 v[80:83], a[104:107], v[104:107], v[80:83]\n"
      "buffer_load_dwordx4 v[140:143], v255, s[48:51], s53 offen offset:1024 sc1\n"
      "v_mfma_f32_16x16x32_bf16 v[84:87], a[108:111], v[104:107], v[84:87]\n"
      "ds_read_b128 v[100:103], v34 offset:58368\n"
      "v_mfma_f32_16x16x32_bf16 v[88:91], a[104:107], v[108:111], v[88:91]\n"
      "v_mfma_f32_16x16x32_bf16 v[92:95], a[108:111], v[108:111], v[92:95]\n"
      "s_waitcnt lgkmcnt(4)\n"
      "ds_read_b128 v[104:107], v35 offset:57344\n"
      "v_mfma_f32_16x16x32_bf16 v[80:83], a[112:115], v[112:115], v[80:83]\n"
      "buffer_load_dwordx4 v[144:147], v255, s[48:51], s53 offen offset:2048 sc1\n"
      "v_mfma_f32_16x16x32_bf16 v[84:87], a[116:119], v[112:115], v[84:87]\n"
      "ds_read_b128 v[108:111], v35 offset:58368\n"
      "v_mfma_f32_16x16x32_bf16 v[88:91], a[112:115], v[116:119], v[88:91]\n"
      "v_mfma_f32_16x16x32_bf16 v[92:95], a[116:119], v[116:119], v[92:95]\n"
      "s_waitcnt lgkmcnt(4)\n"
      "ds_read_b128 v[112:115], v36 offset:57344\n"
      "v_mfma_f32_16x16x32_bf16 v[80:83], a[120:123], v[120:123], v[80:83]\n"
      "buffer_load_dwordx4 v[148:151], v255, s[48:51], s53 offen offset:3072 sc1\n"
      "v_mfma_f32_16x16x32_bf16 v[84:87], a[124:127], v[120:123], v[84:87]\n"
      "ds_read_b128 v[116:119], v36 offset:58368\n"
      "v_mfma_f32_16x16x32_bf16 v[88:91], a[120:123], v[124:127], v[88:91]\n"
      "v_mfma_f32_16x16x32_bf16 v[92:95], a[124:127], v[124:127], v[92:95]\n"
      "s_waitcnt lgkmcnt(4)\n"
      "ds_read_b128 v[120:123], v37 offset:57344\n"
      "v_mfma_f32_16x16x32_bf16 v[80:83], a[128:131], v[96:99], v[80:83]\n"
      "buffer_load_dwordx4 v238, s[16:19], s74 offen offset:2048 lds\n"
      "v_mfma_f32_16x16x32_bf16 v[84:87], a[132:135], v[96:99], v[84:87]\n"
      "s_add_u32 m0, s59, 0xc00\n"
      "ds_read_b128 v[124:127], v37 offset:58368\n"
      "v_mfma_f32_16x16x32_bf16 v[88:91], a[128:131], v[100:103], v[88:91]\n"
      "s_waitcnt vmcnt(7)\n"
      "s_barrier\n"
      "v_mfma_f32_16x16x32_bf16 v[92:95], a[132:135], v[100:103], v[92:95]\n"
      "s_waitcnt lgkmcnt(4)\n"
      "ds_read_b128 v[96:99], v38\n"
      "v_mfma_f32_16x16x32_bf16 v[80:83], a[136:139], v[104:107], v[80:83]\n"
      "v_mfma_f32_16x16x32_bf16 v[84:87], a[140:143], v[104:107], v[84:87]\n"
      "ds_read_b128 v[100:103], v38 offset:1024\n"
      "v_mfma_f32_16x16x32_bf16 v[88:91], a[136:139], v[108:111], v[88:91]\n"
      "buffer_load_dwordx4 v236, s[20:23], s74 offen lds\n"
      "v_mfma_f32_16x16x32_bf16 v[92:95], a[140:143], v[108:111], v[92:95]\n"
      "s_waitcnt lgkmcnt(4)\n"
      "ds_read_b128 v[104:107], v39\n"
      "v_mfma_f32_16x16x32_bf16 v[80:83], a[144:147], v[112:115], v[80:83]\n"
      "v_mfma_f32_16x16x32_bf16 v[84:87], a[148:151], v[112:115], v[84:87]\n"
      "ds_read_b128 v[108:111], v39 offset:1024\n"
      "v_mfma_f32_16x16x32_bf16 v[88:91], a[144:147], v[116:119], v[88:91]\n"
      "v_mfma_f32_16x16x32_bf16 v[92:95], a[148:151], v[116:119], v[92:95]\n"
      "s_waitcnt lgkmcnt(4)\n"
      "ds_read_b128 v[112:115], v40\n"
      "v_mfma_f32_16x16x32_bf16 v[80:83], a[152:155], v[120:123], v[80:83]\n"
      "buffer_load_dwordx4 v237, s[20:23], s74 offen offset:1024 lds\n"
      "v_mfma_f32_16x16x32_bf16 v[84:87], a[156:159], v[120:123], v[84:87]\n"
      "ds_read_b128 v[116:119], v40 offset:1024\n"
      "v_mfma_f32_16x16x32_bf16 v[88:91], a[152:155], v[124:127], v[88:91]\n"
      "v_mfma_f32_16x16x32_bf16 v[92:95], a[156:159], v[124:127], v[92:95]\n"
      "s_waitcnt lgkmcnt(4)\n"
      "ds_read_b128 v[120:123], v41\n"
      "v_mfma_f32_16x16x32_bf16 v[80:83], a[160:163], v[96:99], v[80:83]\n"
      "v_mfma_f32_16x16x32_bf16 v[84:87], a[164:167], v[96:99], v[84:87]\n"
      "ds_read_b128 v[124:127], v41 offset:1024\n"
      "v_mfma_f32_16x16x32_bf16 v[88:91], a[160:163], v[100:103], v[88:91]\n"
      "buffer_load_dwordx4 v238, s[20:23], s74 offen offset:2048 lds\n"
      "v_mfma_f32_16x16x32_bf16 v[92:95], a[164:167], v[100:103], v[92:95]\n"
      "s_waitcnt lgkmcnt(4)\n"
      "ds_read_b128 v[96:99], v38 offset:8192\n"
      "s_add_u32 m0, s59, 0x6000\n"
      "v_mfma_f32_16x16x32_bf16 v[80:83], a[168:171], v[104:107], v[80:83]\n"
      "s_add_u32 s74, s58, 0x2b000\n"
      "v_mfma_f32_16x16x32_bf16 v[84:87], a[172:175], v[104:107], v[84:87]\n"
      "ds_read_b128 v[100:103], v38 offset:9216\n"
      "v_mfma_f32_16x16x32_bf16 v[88:91], a[168:171], v[108:111], v[88:91]\n"
      "v_mfma_f32_16x16x32_bf16 v[92:95], a[172:175], v[108:111], v[92:95]\n"
      "s_waitcnt lgkmcnt(4)\n"
      "ds_read_b128 v[104:107], v39 offset:8192\n"
      "v_mfma_f32_16x16x32_bf16 v[80:83], a[176:179], v[112:115], v[80:83]\n"
      "buffer_load_dwordx4 v[152:155], v255, s[48:51], s54 offen sc1\n"
      "v_mfma_f32_16x16x32_bf16 v[84:87], a[180:183], v[112:115], v[84:87]\n"
      "ds_read_b128 v[108:111], v39 offset:9216\n"
      "v_mfma_f32_16x16x32_bf16 v[88:91], a[176:179], v[116:119], v[88:91]\n"
      "v_mfma_f32_16x16x32_bf16 v[92:95], a[180:183], v[116:119], v[92:95]\n"
      "s_waitcnt lgkmcnt(4)\n"
      "ds_read_b128 v[112:115], v40 offset:8192\n"
      "v_mfma_f32_16x16x32_bf16 v[80:83], a[184:187], v[120:123], v[80:83]\n"
      "v_mfma_f32_16x16x32_bf16 v[84:87], a[188:191], v[120:123], v[84:87]\n"
      "ds_read_b128 v[116:119], v40 offset:9216\n"
      "v_mfma_f32_16x16x32_bf16 v[88:91], a[184:187], v[124:127], v[88:91]\n"
      "buffer_load_dwordx4 v[156:159], v255, s[48:51], s54 offen offset:1024 sc1\n"
      "v_mfma_f32_16x16x32_bf16 v[92:95], a[188:191], v[124:127], v[92:95]\n"
      "s_waitcnt lgkmcnt(4)\n"
      "ds_read_b128 v[120:123], v41 offset:8192\n"
      "v_mfma_f32_16x16x32_bf16 v[80:83], a[192:195], v[96:99], v[80:83]\n"
      "s_waitcnt vmcnt(6)\n"
      "v_max3_u32 v239, v136, v137, v138\n"
      "v_max3_u32 v239, v239, v139, v140\n"
      "v_mfma_f32_16x16x32_bf16 v[84:87], a[196:199], v[96:99], v[84:87]\n"
      "ds_read_b128 v[124:127], v41 offset:9216\n"
      "v_max3_u32 v239, v239, v141, v142\n"
      "v_mfma_f32_16x16x32_bf16 v[88:91], a[192:195], v[100:103], v[88:91]\n"
      "buffer_load_dwordx4 v236, s[16:19], s74 offen lds\n"
      "v_mfma_f32_16x16x32_bf16 v[92:95], a[196:199], v[100:103], v[92:95]\n"
      "s_waitcnt lgkmcnt(4)\n"
      "ds_read_b128 v[96:99], v38 offset:16384\n"
      "v_max3_u32 v239, v239, v143, v144\n"
      "v_mfma_f32_16x16x32_bf16 v[80:83], a[200:203], v[104:107], v[80:83]\n"
      "v_max3_u32 v239, v239, v145, v146\n"
      "v_max3_u32 v239, v239, v147, v148\n"
      "v_mfma_f32_16x16x32_bf16 v[84:87], a[204:207], v[104:107], v[84:87]\n"
      "ds_read_b128 v[100:103], v38 offset:17408\n"
      "v_max3_u32 v239, v239, v149, v150\n"
      "v_mfma_f32_16x16x32_bf16 v[88:91], a[200:203], v[108:111], v[88:91]\n"
      "v_max_u32 v239, v239, v151\n"
      "v_cmp_eq_u32 vcc, -1, v239\n"
      "s_nop 0\n"
      "s_cbranch_vccnz L7_%=\n"
      "L8_%=:\n"
      "v_mfma_f32_16x16x32_bf16 v[92:95], a[204:207], v[108:111], v[92:95]\n"
      "s_waitcnt lgkmcnt(4)\n"
      "ds_read_b128 v[104:107], v39 offset:16384\n"
      "v_lshlrev_b32 v10, 16, v136\n"
      "v_mfma_f32_16x16x32_bf16 v[80:83], a[208:211], v[112:115], v[80:83]\n"
      "buffer_load_dwordx4 v237, s[16:19], s74 offen offset:1024 lds\n"
      "v_mfma_f32_16x16x32_bf16 v[84:87], a[212:215], v[112:115], v[84:87]\n"
      "ds_read_b128 v[108:111], v39 offset:17408\n"
      "v_and_b32 v11, 0xffff0000, v136\n"
      "v_mfma_f32_16x16x32_bf16 v[88:91], a[208:211], v[116:119], v[88:91]\n"
      "v_lshlrev_b32 v48, 16, v144\n"
      "v_and_b32 v49, 0xffff0000, v144\n"
      "v_mfma_f32_16x16x32_bf16 v[92:95], a[212:215], v[116:119], v[92:95]\n"
      "s_waitcnt lgkmcnt(4)\n"
      "ds_read_b128 v[112:115], v40 offset:16384\n"
      "v_add_f32 v10, v10, v48\n"
      "v_mfma_f32_16x16x32_bf16 v[80:83], a[216:219], v[120:123], v[80:83]\n"
      "v_add_f32 v11, v11, v49\n"
      "v_lshlrev_b32 v12, 16, v137\n"
      "v_mfma_f32_16x16x32_bf16 v[84:87], a[220:223], v[120:123], v[84:87]\n"
      "ds_read_b128 v[116:119], v40 offset:17408\n"
      "v_and_b32 v13, 0xffff0000, v137\n"
      "v_mfma_f32_16x16x32_bf16 v[88:91], a[216:219], v[124:127], v[88:91]\n"
      "buffer_load_dwordx4 v238, s[16:19], s74 offen offset:2048 lds\n"
      "v_mfma_f32_16x16x32_bf16 v[92:95], a[220:223], v[124:127], v[92:95]\n"
      "s_waitcnt lgkmcnt(4)\n"
      "ds_read_b128 v[120:123], v41 offset:16384\n"
      "s_add_u32 m0, s59, 0x6c00\n"
      "v_mfma_f32_16x16x32_bf16 v[80:83], a[224:227], v[96:99], v[80:83]\n"
      "v_lshlrev_b32 v50, 16, v145\n"
      "v_and_b32 v51, 0xffff0000, v145\n"
      "v_mfma_f32_16x16x32_bf16 v[84:87], a[228:231], v[96:99], v[84:87]\n"
      "ds_read_b128 v[124:127], v41 offset:17408\n"
      "v_add_f32 v12, v12, v50\n"
      "v_mfma_f32_16x16x32_bf16 v[88:91], a[224:227], v[100:103], v[88:91]\n"
      "v_add_f32 v13, v13, v51\n"
      "v_lshlrev_b32 v14, 16, v138\n"
      "v_mfma_f32_16x16x32_bf16 v[92:95], a[228:231], v[100:103], v[92:95]\n"
      "s_waitcnt lgkmcnt(4)\n"
      "buffer_load_dwordx4 v236, s[20:23], s74 offen lds\n"
      "v_mfma_f32_16x16x32_bf16 v[80:83], a[232:235], v[104:107], v[80:83]\n"
      "v_and_b32 v15, 0xffff0000, v138\n"
      "v_lshlrev_b32 v52, 16, v146\n"
      "v_mfma_f32_16x16x32_bf16 v[84:87], a[236:239], v[104:107], v[84:87]\n"
      "v_and_b32 v53, 0xffff0000, v146\n"
      "v_add_f32 v14, v14, v52\n"
      "v_mfma_f32_16x16x32_bf16 v[88:91], a[232:235], v[108:111], v[88:91]\n"
      "v_add_f32 v15, v15, v53\n"
      "v_lshlrev_b32 v16, 16, v139\n"
      "v_mfma_f32_16x16x32_bf16 v[92:95], a[236:239], v[108:111], v[92:95]\n"
      "s_waitcnt lgkmcnt(2)\n"
      "v_and_b32 v17, 0xffff0000, v139\n"
      "v_lshlrev_b32 v54, 16, v147\n"
      "v_mfma_f32_16x16x32_bf16 v[80:83], a[240:243], v[112:115], v[80:83]\n"
      "v_and_b32 v55, 0xffff0000, v147\n"
      "v_add_f32 v16, v16, v54\n"
      "v_mfma_f32_16x16x32_bf16 v[84:87], a[244:247], v[112:115], v[84:87]\n"
      "buffer_load_dwordx4 v237, s[20:23], s74 offen offset:1024 lds\n"
      "v_mfma_f32_16x16x32_bf16 v[88:91], a[240:243], v[116:119], v[88:91]\n"
      "v_add_f32 v17, v17, v55\n"
      "v_lshlrev_b32 v18, 16, v140\n"
      "v_mfma_f32_16x16x32_bf16 v[92:95], a[244:247], v[116:119], v[92:95]\n"
      "s_waitcnt lgkmcnt(0)\n"
      "v_and_b32 v19, 0xffff0000, v140\n"
      "v_lshlrev_b32 v56, 16, v148\n"
      "v_mfma_f32_16x16x32_bf16 v[80:83], a[248:251], v[120:123], v[80:83]\n"
      "v_and_b32 v57, 0xffff0000, v148\n"
      "v_add_f32 v18, v18, v56\n"
      "v_mfma_f32_16x16x32_bf16 v[84:87], a[252:255], v[120:123], v[84:87]\n"
      "v_add_f32 v19, v19, v57\n"
      "v_lshlrev_b32 v20, 16, v141\n"
      "v_mfma_f32_16x16x32_bf16 v[88:91], a[248:251], v[124:127], v[88:91]\n"
      "v_and_b32 v21, 0xffff0000, v141\n"
      "v_lshlrev_b32 v58, 16, v149\n"
      "v_mfma_f32_16x16x32_bf16 v[92:95], a[252:255], v[124:127], v[92:95]\n"
      "buffer_load_dwordx4 v238, s[20:23], s74 offen offset:2048 lds\n"
      "s_barrier\n"
      "s_add_u32 m0, s59, 0xc000\n"
      "s_add_u32 s74, s58, 0x56000\n"
      "buffer_load_dwordx4 v236, s[16:19], s74 offen lds\n"
      "v_and_b32 v59, 0xffff0000, v149\n"
      "v_add_f32 v20, v20, v58\n"
      "v_add_f32 v21, v21, v59\n"
      "v_lshlrev_b32 v22, 16, v142\n"
      "v_and_b32 v23, 0xffff0000, v142\n"
      "v_lshlrev_b32 v60, 16, v150\n"
      "v_and_b32 v61, 0xffff0000, v150\n"
      "v_add_f32 v22, v22, v60\n"
      "v_add_f32 v23, v23, v61\n"
      "v_lshlrev_b32 v24, 16, v143\n"
      "v_and_b32 v25, 0xffff0000, v143\n"
      "v_lshlrev_b32 v62, 16, v151\n"
      "v_and_b32 v63, 0xffff0000, v151\n"
      "v_add_f32 v24, v24, v62\n"
      "v_add_f32 v25, v25, v63\n"
      "v_pk_add_f32 v[80:81], v[80:81], v[10:11]\n"
      "v_pk_add_f32 v[82:83], v[82:83], v[12:13]\n"
      "v_pk_add_f32 v[84:85], v[84:85], v[14:15]\n"
      "v_pk_add_f32 v[86:87], v[86:87], v[16:17]\n"
      "v_pk_add_f32 v[88:89], v[88:89], v[18:19]\n"
      "v_pk_add_f32 v[90:91], v[90:91], v[20:21]\n"
      "v_pk_add_f32 v[92:93], v[92:93], v[22:23]\n"
      "v_pk_add_f32 v[94:95], v[94:95], v[24:25]\n"
      "buffer_load_dwordx4 v237, s[16:19], s74 offen offset:1024 lds\n"
      "v_mov_b32 v56, 0\n"
      "v_mov_b32 v57, 0\n"
      "v_mov_b32 v58, 0\n"
      "v_mov_b32 v59, 0\n"
      "s_waitcnt vmcnt(8)\n"
      "v_max3_u32 v239, v152, v153, v154\n"
      "v_max3_u32 v239, v239, v155, v156\n"
      "v_max3_u32 v239, v239, v157, v158\n"
      "v_max_u32 v239, v239, v159\n"
      "v_cmp_eq_u32 vcc, -1, v239\n"
      "s_nop 0\n"
      "s_cbranch_vccnz L9_%=\n"
      "L10_%=:\n"
      "v_lshlrev_b32 v48, 16, v152\n"
      "v_and_b32 v49, 0xffff0000, v152\n"
      "v_lshlrev_b32 v50, 16, v153\n"
      "v_and_b32 v51, 0xffff0000, v153\n"
      "v_lshlrev_b32 v52, 16, v154\n"
      "v_and_b32 v53, 0xffff0000, v154\n"
      "v_lshlrev_b32 v54, 16, v155\n"
      "v_and_b32 v55, 0xffff0000, v155\n"
      "v_pk_add_f32 v[80:81], v[80:81], v[48:49]\n"
      "v_pk_add_f32 v[82:83], v[82:83], v[50:51]\n"
      "v_pk_add_f32 v[84:85], v[84:85], v[52:53]\n"
      "v_pk_add_f32 v[86:87], v[86:87], v[54:55]\n"
      "v_cvt_pk_bf16_f32 v168, v80, v81\n"
      "v_cvt_pk_bf16_f32 v169, v82, v83\n"
      "v_cvt_pk_bf16_f32 v170, v84, v85\n"
      "v_cvt_pk_bf16_f32 v171, v86, v87\n"
      "buffer_store_dwordx4 v[168:171], v255, s[48:51], s56 offen sc1\n"
      "v_lshlrev_b32 v48, 16, v156\n"
      "v_and_b32 v49, 0xffff0000, v156\n"
      "v_lshlrev_b32 v50, 16, v157\n"
      "v_and_b32 v51, 0xffff0000, v157\n"
      "v_lshlrev_b32 v52, 16, v158\n"
      "v_and_b32 v53, 0xffff0000, v158\n"
      "v_lshlrev_b32 v54, 16, v159\n"
      "v_and_b32 v55, 0xffff0000, v159\n"
      "v_pk_add_f32 v[88:89], v[88:89], v[48:49]\n"
      "v_pk_add_f32 v[90:91], v[90:91], v[50:51]\n"
      "v_pk_add_f32 v[92:93], v[92:93], v[52:53]\n"
      "v_pk_add_f32 v[94:95], v[94:95], v[54:55]\n"
      "v_cvt_pk_bf16_f32 v172, v88, v89\n"
      "v_cvt_pk_bf16_f32 v173, v90, v91\n"
      "v_cvt_pk_bf16_f32 v174, v92, v93\n"
      "v_cvt_pk_bf16_f32 v175, v94, v95\n"
      "buffer_store_dwordx4 v[172:175], v255, s[48:51], s56 offen offset:1024 sc1\n"
      "v_dot2c_f32_bf16 v56, v168, v248\n"
      "v_dot2c_f32_bf16 v57, v168, v168\n"
      "v_dot2c_f32_bf16 v58, v172, v248\n"
      "v_dot2c_f32_bf16 v59, v172, v172\n"
      "v_dot2c_f32_bf16 v56, v169, v248\n"
      "v_dot2c_f32_bf16 v57, v169, v169\n"
      "v_dot2c_f32_bf16 v58, v173, v248\n"
      "v_dot2c_f32_bf16 v59, v173, v173\n"
      "v_dot2c_f32_bf16 v56, v170, v248\n"
      "v_dot2c_f32_bf16 v57, v170, v170\n"
      "v_dot2c_f32_bf16 v58, v174, v248\n"
      "v_dot2c_f32_bf16 v59, v174, v174\n"
      "v_dot2c_f32_bf16 v56, v171, v248\n"
      "v_dot2c_f32_bf16 v57, v171, v171\n"
      "v_dot2c_f32_bf16 v58, v175, v248\n"
      "v_dot2c_f32_bf16 v59, v175, v175\n"
      "s_nop 1\n"
      "v_permlane32_swap_b32 v56, v58\n"
      "v_permlane32_swap_b32 v57, v59\n"
      "v_add_f32 v56, v56, v58\n"
      "v_add_f32 v57, v57, v59\n"
      "s_nop 1\n"
      "v_permlane16_swap_b32 v56, v57\n"
      "v_add_f32 v56, v56, v57\n"
      "buffer_store_dword v56, v42, s[48:51], s57 offen sc1\n"
      "v_mov_b32 v210, 0\n"
      "v_mov_b32 v211, 0\n"
      "v_mov_b32 v212, 0\n"
      "v_mov_b32 v213, 0\n"
      "v_mov_b32 v214, 0\n"
      "v_mov_b32 v215, 0\n"
      "buffer_load_dwordx4 v238, s[16:19], s74 offen offset:2048 lds\n"
      "s_add_u32 m0, s59, 0xcc00\n"
      "s_nop 0\n"
      "buffer_load_dwordx4 v236, s[20:23], s74 offen lds\n"
      "buffer_load_dwordx4 v237, s[20:23], s74 offen offset:1024 lds\n"
      "buffer_load_dwordx4 v238, s[20:23], s74 offen offset:2048 lds\n"
      "ds_read_b64_tr_b16 v[48:49], v224\n"
      "ds_read_b64_tr_b16 v[50:51], v224 offset:768\n"
      "ds_read_b64_tr_b16 v[52:53], v224 offset:3072\n"
      "ds_read_b64_tr_b16 v[54:55], v224 offset:3840\n"
      "ds_read_b64_tr_b16 v[56:57], v224 offset:64\n"
      "ds_read_b64_tr_b16 v[58:59], v224 offset:832\n"
      "ds_read_b64_tr_b16 v[60:61], v224 offset:3136\n"
      "ds_read_b64_tr_b16 v[62:63], v224 offset:3904\n"
      "ds_read_b64_tr_b16 v[64:65], v224 offset:128\n"
      "ds_read_b64_tr_b16 v[66:67], v224 offset:896\n"
      "ds_read_b64_tr_b16 v[68:69], v224 offset:3200\n"
      "ds_read_b64_tr_b16 v[70:71], v224 offset:3968\n"
      "s_waitcnt lgkmcnt(11)\n"
      "v_dot2c_f32_bf16 v210, v48, v248\n"
      "s_waitcnt lgkmcnt(9)\n"
      "v_dot2c_f32_bf16 v211, v52, v248\n"
      "s_waitcnt lgkmcnt(7)\n"
      "v_dot2c_f32_bf16 v212, v56, v248\n"
      "s_waitcnt lgkmcnt(5)\n"
      "v_dot2c_f32_bf16 v213, v60, v248\n"
      "s_waitcnt lgkmcnt(3)\n"
      "v_dot2c_f32_bf16 v214, v64, v248\n"
      "s_waitcnt lgkmcnt(1)\n"
      "v_dot2c_f32_bf16 v215, v68, v248\n"
      "s_add_u32 m0, s59, 0x12000\n"
      "s_add_u32 s74, s58, 0x81000\n"
      "buffer_load_dwordx4 v236, s[16:19], s74 offen lds\n"
      "v_dot2c_f32_bf16 v210, v49, v248\n"
      "v_dot2c_f32_bf16 v211, v53, v248\n"
      "v_dot2c_f32_bf16 v212, v57, v248\n"
      "v_dot2c_f32_bf16 v213, v61, v248\n"
      "v_dot2c_f32_bf16 v214, v65, v248\n"
      "v_dot2c_f32_bf16 v215, v69, v248\n"
      "v_dot2c_f32_bf16 v210, v50, v248\n"
      "v_dot2c_f32_bf16 v211, v54, v248\n"
      "v_dot2c_f32_bf16 v212, v58, v248\n"
      "v_dot2c_f32_bf16 v213, v62, v248\n"
      "v_dot2c_f32_bf16 v214, v66, v248\n"
      "s_waitcnt lgkmcnt(0)\n"
      "v_dot2c_f32_bf16 v215, v70, v248\n"
      "buffer_load_dwordx4 v237, s[16:19], s74 offen offset:1024 lds\n"
      "v_dot2c_f32_bf16 v210, v51, v248\n"
      "v_dot2c_f32_bf16 v211, v55, v248\n"
      "v_dot2c_f32_bf16 v212, v59, v248\n"
      "v_dot2c_f32_bf16 v213, v63, v248\n"
      "v_dot2c_f32_bf16 v214, v67, v248\n"
      "v_dot2c_f32_bf16 v215, v71, v248\n"
      "s_waitcnt vmcnt(6)\n"
      "s_barrier\n"
      "s_add_u32 s70, s9, 0x108800\n"
      "buffer_store_dwordx2 v[246:247], v245, s[48:51], s70 offen sc1\n"
      "ds_read_b64_tr_b16 v[72:73], v224 offset:24576\n"
      "ds_read_b64_tr_b16 v[74:75], v224 offset:25344\n"
      "ds_read_b64_tr_b16 v[76:77], v224 offset:27648\n"
      "ds_read_b64_tr_b16 v[78:79], v224 offset:28416\n"
      "ds_read_b64_tr_b16 v[80:81], v224 offset:24640\n"
      "ds_read_b64_tr_b16 v[82:83], v224 offset:25408\n"
      "ds_read_b64_tr_b16 v[84:85], v224 offset:27712\n"
      "ds_read_b64_tr_b16 v[86:87], v224 offset:28480\n"
      "ds_read_b64_tr_b16 v[88:89], v224 offset:24704\n"
      "ds_read_b64_tr_b16 v[90:91], v224 offset:25472\n"
      "ds_read_b64_tr_b16 v[92:93], v224 offset:27776\n"
      "ds_read_b64_tr_b16 v[94:95], v224 offset:28544\n"
      "s_waitcnt lgkmcnt(11)\n"
      "v_dot2c_f32_bf16 v210, v72, v248\n"
      "s_waitcnt lgkmcnt(9)\n"
      "v_dot2c_f32_bf16 v211, v76, v248\n"
      "s_waitcnt lgkmcnt(7)\n"
      "v_dot2c_f32_bf16 v212, v80, v248\n"
      "s_waitcnt lgkmcnt(5)\n"
      "v_dot2c_f32_bf16 v213, v84, v248\n"
      "s_waitcnt lgkmcnt(3)\n"
      "v_dot2c_f32_bf16 v214, v88, v248\n"
      "s_waitcnt lgkmcnt(1)\n"
      "v_dot2c_f32_bf16 v215, v92, v248\n"
      "buffer_load_dwordx4 v238, s[16:19], s74 offen offset:2048 lds\n"
      "v_dot2c_f32_bf16 v210, v73, v248\n"
      "v_dot2c_f32_bf16 v211, v77, v248\n"
      "v_dot2c_f32_bf16 v212, v81, v248\n"
      "v_dot2c_f32_bf16 v213, v85, v248\n"
      "v_dot2c_f32_bf16 v214, v89, v248\n"
      "v_dot2c_f32_bf16 v215, v93, v248\n"
      "v_dot2c_f32_bf16 v210, v74, v248\n"
      "v_dot2c_f32_bf16 v211, v78, v248\n"
      "v_dot2c_f32_bf16 v212, v82, v248\n"
      "v_dot2c_f32_bf16 v213, v86, v248\n"
      "v_dot2c_f32_bf16 v214, v90, v248\n"
      "s_waitcnt lgkmcnt(0)\n"
      "v_dot2c_f32_bf16 v215, v94, v248\n"
      "s_add_u32 m0, s59, 0x12c00\n"
      "s_nop 0\n"
      "buffer_load_dwordx4 v236, s[20:23], s74 offen lds\n"
      "v_dot2c_f32_bf16 v210, v75, v248\n"
      "v_dot2c_f32_bf16 v211, v79, v248\n"
      "v_dot2c_f32_bf16 v212, v83, v248\n"
      "v_dot2c_f32_bf16 v213, v87, v248\n"
      "v_dot2c_f32_bf16 v214, v91, v248\n"
      "v_dot2c_f32_bf16 v215, v95, v248\n"
      "buffer_load_dwordx4 v237, s[20:23], s74 offen offset:1024 lds\n"
      "s_waitcnt vmcnt(6)\n"
      "ds_read_b64_tr_b16 v[48:49], v225\n"
      "ds_read_b64_tr_b16 v[50:51], v225 offset:768\n"
      "ds_read_b64_tr_b16 v[52:53], v225 offset:3072\n"
      "ds_read_b64_tr_b16 v[54:55], v225 offset:3840\n"
      "ds_read_b64_tr_b16 v[56:57], v225 offset:64\n"
      "ds_read_b64_tr_b16 v[58:59], v225 offset:832\n"
      "ds_read_b64_tr_b16 v[60:61], v225 offset:3136\n"
      "ds_read_b64_tr_b16 v[62:63], v225 offset:3904\n"
      "ds_read_b64_tr_b16 v[64:65], v225 offset:128\n"
      "ds_read_b64_tr_b16 v[66:67], v225 offset:896\n"
      "ds_read_b64_tr_b16 v[68:69], v225 offset:3200\n"
      "ds_read_b64_tr_b16 v[70:71], v225 offset:3968\n"
      "s_waitcnt lgkmcnt(11)\n"
      "v_dot2c_f32_bf16 v210, v48, v248\n"
      "s_waitcnt lgkmcnt(9)\n"
      "v_dot2c_f32_bf16 v211, v52, v248\n"
      "s_waitcnt lgkmcnt(7)\n"
      "v_dot2c_f32_bf16 v212, v56, v248\n"
      "s_waitcnt lgkmcnt(5)\n"
      "v_dot2c_f32_bf16 v213, v60, v248\n"
      "s_waitcnt lgkmcnt(3)\n"
      "v_dot2c_f32_bf16 v214, v64, v248\n"
      "s_waitcnt lgkmcnt(1)\n"
      "v_dot2c_f32_bf16 v215, v68, v248\n"
      "buffer_load_dwordx4 v238, s[20:23], s74 offen offset:2048 lds\n"
      "v_dot2c_f32_bf16 v210, v49, v248\n"
      "v_dot2c_f32_bf16 v211, v53, v248\n"
      "v_dot2c_f32_bf16 v212, v57, v248\n"
      "v_dot2c_f32_bf16 v213, v61, v248\n"
      "v_dot2c_f32_bf16 v214, v65, v248\n"
      "v_dot2c_f32_bf16 v215, v69, v248\n"
      "v_dot2c_f32_bf16 v210, v50, v248\n"
      "v_dot2c_f32_bf16 v211, v54, v248\n"
      "v_dot2c_f32_bf16 v212, v58, v248\n"
      "v_dot2c_f32_bf16 v213, v62, v248\n"
      "v_dot2c_f32_bf16 v214, v66, v248\n"
      "s_waitcnt lgkmcnt(0)\n"
      "v_dot2c_f32_bf16 v215, v70, v248\n"
      "s_add_u32 m0, s59, 0x18000\n"
      "s_add_u32 s74, s58, 0xac000\n"
      "buffer_load_dwordx4 v236, s[16:19], s74 offen lds\n"
      "v_dot2c_f32_bf16 v210, v51, v248\n"
      "v_dot2c_f32_bf16 v211, v55, v248\n"
      "v_dot2c_f32_bf16 v212, v59, v248\n"
      "v_dot2c_f32_bf16 v213, v63, v248\n"
      "v_dot2c_f32_bf16 v214, v67, v248\n"
      "v_dot2c_f32_bf16 v215, v71, v248\n"
      "buffer_load_dwordx4 v237, s[16:19], s74 offen offset:1024 lds\n"
      "buffer_load_dwordx4 v238, s[16:19], s74 offen offset:2048 lds\n"
      "s_add_u32 m0, s59, 0x18c00\n"
      "s_nop 0\n"
      "buffer_load_dwordx4 v236, s[20:23], s74 offen lds\n"
      "buffer_load_dwordx4 v237, s[20:23], s74 offen offset:1024 lds\n"
      "s_waitcnt vmcnt(5)\n"
      "ds_read_b64_tr_b16 v[72:73], v225 offset:24576\n"
      "ds_read_b64_tr_b16 v[74:75], v225 offset:25344\n"
      "ds_read_b64_tr_b16 v[76:77], v225 offset:27648\n"
      "ds_read_b64_tr_b16 v[78:79], v225 offset:28416\n"
      "ds_read_b64_tr_b16 v[80:81], v225 offset:24640\n"
      "ds_read_b64_tr_b16 v[82:83], v225 offset:25408\n"
      "ds_read_b64_tr_b16 v[84:85], v225 offset:27712\n"
      "ds_read_b64_tr_b16 v[86:87], v225 offset:28480\n"
      "ds_read_b64_tr_b16 v[88:89], v225 offset:24704\n"
      "ds_read_b64_tr_b16 v[90:91], v225 offset:25472\n"
      "ds_read_b64_tr_b16 v[92:93], v225 offset:27776\n"
      "ds_read_b64_tr_b16 v[94:95], v225 offset:28544\n"
      "s_waitcnt lgkmcnt(11)\n"
      "v_dot2c_f32_bf16 v210, v72, v248\n"
      "s_waitcnt lgkmcnt(9)\n"
      "v_dot2c_f32_bf16 v211, v76, v248\n"
      "s_waitcnt lgkmcnt(7)\n"
      "v_dot2c_f32_bf16 v212, v80, v248\n"
      "s_waitcnt lgkmcnt(5)\n"
      "v_dot2c_f32_bf16 v213, v84, v248\n"
      "s_waitcnt lgkmcnt(3)\n"
      "v_dot2c_f32_bf16 v214, v88, v248\n"
      "s_waitcnt lgkmcnt(1)\n"
      "v_dot2c_f32_bf16 v215, v92, v248\n"
      "buffer_load_dwordx4 v238, s[20:23], s74 offen offset:2048 lds\n"
      "v_dot2c_f32_bf16 v210, v73, v248\n"
      "v_dot2c_f32_bf16 v211, v77, v248\n"
      "v_dot2c_f32_bf16 v212, v81, v248\n"
      "v_dot2c_f32_bf16 v213, v85, v248\n"
      "v_dot2c_f32_bf16 v214, v89, v248\n"
      "v_dot2c_f32_bf16 v215, v93, v248\n"
      "v_dot2c_f32_bf16 v210, v74, v248\n"
      "v_dot2c_f32_bf16 v211, v78, v248\n"
      "v_dot2c_f32_bf16 v212, v82, v248\n"
      "v_dot2c_f32_bf16 v213, v86, v248\n"
      "v_dot2c_f32_bf16 v214, v90, v248\n"
      "s_waitcnt lgkmcnt(0)\n"
      "v_dot2c_f32_bf16 v215, v94, v248\n"
      "s_add_u32 m0, s59, 0x1e000\n"
      "s_add_u32 s74, s58, 0xd7000\n"
      "buffer_load_dwordx4 v236, s[16:19], s74 offen lds\n"
      "v_dot2c_f32_bf16 v210, v75, v248\n"
      "v_dot2c_f32_bf16 v211, v79, v248\n"
      "v_dot2c_f32_bf16 v212, v83, v248\n"
      "v_dot2c_f32_bf16 v213, v87, v248\n"
      "v_dot2c_f32_bf16 v214, v91, v248\n"
      "v_dot2c_f32_bf16 v215, v95, v248\n"
      "buffer_load_dwordx4 v237, s[16:19], s74 offen offset:1024 lds\n"
      "s_mov_b32 s72, 0x108800\n"
      "buffer_load_dwordx2 v[242:243], v240, s[48:51], s72 offen sc1\n"
      "buffer_load_dwordx4 v238, s[16:19], s74 offen offset:2048 lds\n"
      "s_add_u32 m0, s59, 0x1ec00\n"
      "s_nop 0\n"
      "buffer_load_dwordx4 v236, s[20:23], s74 offen lds\n"
      "buffer_load_dwordx4 v237, s[20:23], s74 offen offset:1024 lds\n"
      "buffer_load_dwordx4 v238, s[20:23], s74 offen offset:2048 lds\n"
      "s_waitcnt vmcnt(4)\n"
      "v_cmp_ne_u64 vcc, s[60:61], v[242:243]\n"
      "s_nop 0\n"
      "s_cbranch_vccnz L11_%=\n"
      "L12_%=:\n"
      "buffer_load_dwordx4 a[128:131], v255, s[48:51], s75 offen sc1\n"
      "buffer_load_dwordx4 a[132:135], v255, s[48:51], s75 offen offset:1024 sc1\n"
      "buffer_load_dwordx4 a[136:139], v255, s[48:51], s75 offen offset:2048 sc1\n"
      "buffer_load_dwordx4 a[140:143], v255, s[48:51], s75 offen offset:3072 sc1\n"
      "s_add_u32 s75, s75, 0x1000\n"
      "buffer_load_dwordx4 a[144:147], v255, s[48:51], s75 offen sc1\n"
      "buffer_load_dwordx4 a[148:151], v255, s[48:51], s75 offen offset:1024 sc1\n"
      "buffer_load_dwordx4 a[152:155], v255, s[48:51], s75 offen offset:2048 sc1\n"
      "buffer_load_dwordx4 a[156:159], v255, s[48:51], s75 offen offset:3072 sc1\n"
      "ds_read_b64_tr_b16 v[48:49], v226\n"
      "ds_read_b64_tr_b16 v[50:51], v226 offset:768\n"
      "ds_read_b64_tr_b16 v[52:53], v226 offset:3072\n"
      "ds_read_b64_tr_b16 v[54:55], v226 offset:3840\n"
      "ds_read_b64_tr_b16 v[56:57], v226 offset:64\n"
      "ds_read_b64_tr_b16 v[58:59], v226 offset:832\n"
      "ds_read_b64_tr_b16 v[60:61], v226 offset:3136\n"
      "ds_read_b64_tr_b16 v[62:63], v226 offset:3904\n"
      "ds_read_b64_tr_b16 v[64:65], v226 offset:128\n"
      "ds_read_b64_tr_b16 v[66:67], v226 offset:896\n"
      "ds_read_b64_tr_b16 v[68:69], v226 offset:3200\n"
      "ds_read_b64_tr_b16 v[70:71], v226 offset:3968\n"
      "s_waitcnt vmcnt(8)\n"
      "ds_read_b64_tr_b16 v[72:73], v226 offset:24576\n"
      "ds_read_b64_tr_b16 v[74:75], v226 offset:25344\n"
      "ds_read_b64_tr_b16 v[76:77], v226 offset:27648\n"
      "s_waitcnt lgkmcnt(7)\n"
      "ds_read_b64_tr_b16 v[78:79], v226 offset:28416\n"
      "ds_read_b64_tr_b16 v[80:81], v226 offset:24640\n"
      "ds_read_b64_tr_b16 v[82:83], v226 offset:25408\n"
      "ds_read_b64_tr_b16 v[84:85], v226 offset:27712\n"
      "ds_read_b64_tr_b16 v[86:87], v226 offset:28480\n"
      "ds_read_b64_tr_b16 v[88:89], v226 offset:24704\n"
      "ds_read_b64_tr_b16 v[90:91], v226 offset:25472\n"
      "ds_read_b64_tr_b16 v[92:93], v226 offset:27776\n"
      "s_waitcnt lgkmcnt(7)\n"
      "ds_read_b64_tr_b16 v[94:95], v226 offset:28544\n"
      "s_mul_i32 s71, s63, 0x300\n"
      "s_add_u32 s71, s71, 0x26000\n"
      "v_lshl_add_u32 v209, v0, 2, s71\n"
      "v_lshl_add_u32 v46, v249, 4, s71\n"
      "v_lshlrev_b32 v208, 2, v0\n"
      "v_add_u32 v208, 0x26000, v208\n"
      "v_dot2c_f32_bf16 v210, v48, v248\n"
      "v_dot2c_f32_bf16 v211, v52, v248\n"
      "v_dot2c_f32_bf16 v212, v56, v248\n"
      "v_dot2c_f32_bf16 v213, v60, v248\n"
      "v_dot2c_f32_bf16 v214, v64, v248\n"
      "v_dot2c_f32_bf16 v215, v68, v248\n"
      "v_dot2c_f32_bf16 v210, v49, v248\n"
      "v_dot2c_f32_bf16 v211, v53, v248\n"
      "v_dot2c_f32_bf16 v212, v57, v248\n"
      "v_dot2c_f32_bf16 v213, v61, v248\n"
      "v_dot2c_f32_bf16 v214, v65, v248\n"
      "v_dot2c_f32_bf16 v215, v69, v248\n"
      "v_dot2c_f32_bf16 v210, v50, v248\n"
      "v_dot2c_f32_bf16 v211, v54, v248\n"
      "v_dot2c_f32_bf16 v212, v58, v248\n"
      "v_dot2c_f32_bf16 v213, v62, v248\n"
      "v_dot2c_f32_bf16 v214, v66, v248\n"
      "v_dot2c_f32_bf16 v215, v70, v248\n"
      "v_dot2c_f32_bf16 v210, v51, v248\n"
      "v_dot2c_f32_bf16 v211, v55, v248\n"
      "v_dot2c_f32_bf16 v212, v59, v248\n"
      "v_dot2c_f32_bf16 v213, v63, v248\n"
      "v_dot2c_f32_bf16 v214, v67, v248\n"
      "v_dot2c_f32_bf16 v215, v71, v248\n"
      "s_barrier\n"
      "ds_read_b64_tr_b16 v[160:161], v230\n"
      "ds_read_b64_tr_b16 v[162:163], v230 offset:768\n"
      "ds_read_b64_tr_b16 v[164:165], v227\n"
      "ds_read_b64_tr_b16 v[166:167], v227 offset:768\n"
      "ds_read_b64_tr_b16 v[168:169], v230 offset:3072\n"
      "ds_read_b64_tr_b16 v[170:171], v230 offset:3840\n"
      "ds_read_b64_tr_b16 v[172:173], v227 offset:3072\n"
      "s_waitcnt lgkmcnt(7)\n"
      "ds_read_b64_tr_b16 v[174:175], v227 offset:3840\n"
      "ds_read_b64_tr_b16 v[176:177], v230 offset:64\n"
      "ds_read_b64_tr_b16 v[178:179], v230 offset:832\n"
      "ds_read_b64_tr_b16 v[180:181], v227 offset:64\n"
      "ds_read_b64_tr_b16 v[182:183], v227 offset:832\n"
      "v_dot2c_f32_bf16 v210, v72, v248\n"
      "v_dot2c_f32_bf16 v211, v76, v248\n"
      "v_dot2c_f32_bf16 v212, v80, v248\n"
      "v_dot2c_f32_bf16 v213, v84, v248\n"
      "v_dot2c_f32_bf16 v214, v88, v248\n"
      "v_dot2c_f32_bf16 v215, v92, v248\n"
      "v_dot2c_f32_bf16 v210, v73, v248\n"
      "v_dot2c_f32_bf16 v211, v77, v248\n"
      "v_dot2c_f32_bf16 v212, v81, v248\n"
      "v_dot2c_f32_bf16 v213, v85, v248\n"
      "v_dot2c_f32_bf16 v214, v89, v248\n"
      "v_dot2c_f32_bf16 v215, v93, v248\n"
      "v_dot2c_f32_bf16 v210, v74, v248\n"
      "v_dot2c_f32_bf16 v211, v78, v248\n"
      "v_dot2c_f32_bf16 v212, v82, v248\n"
      "v_dot2c_f32_bf16 v213, v86, v248\n"
      "v_dot2c_f32_bf16 v214, v90, v248\n"
      "v_dot2c_f32_bf16 v215, v94, v248\n"
      "v_dot2c_f32_bf16 v210, v75, v248\n"
      "v_dot2c_f32_bf16 v211, v79, v248\n"
      "v_dot2c_f32_bf16 v212, v83, v248\n"
      "v_dot2c_f32_bf16 v213, v87, v248\n"
      "v_dot2c_f32_bf16 v214, v91, v248\n"
      "v_dot2c_f32_bf16 v215, v95, v248\n"
      "s_waitcnt vmcnt(6) lgkmcnt(8)\n"
      "v_mfma_f32_16x16x32_bf16 v[2:5], v[160:163], a[128:131], 0\n"
      "s_add_u32 s75, s75, 0x3000\n"
      "buffer_load_dwordx4 a[160:163], v255, s[48:51], s75 offen sc1\n"
      "v_mfma_f32_16x16x32_bf16 v[10:13], v[160:163], a[132:135], 0\n"
      "ds_read_b64_tr_b16 v[184:185], v230 offset:3136\n"
      "ds_read_b64_tr_b16 v[186:187], v230 offset:3904\n"
      "v_mfma_f32_16x16x32_bf16 v[6:9], v[164:167], a[128:131], 0\n"
      "v_mfma_f32_16x16x32_bf16 v[14:17], v[164:167], a[132:135], 0\n"
      "s_waitcnt lgkmcnt(6)\n"
      "ds_read_b64_tr_b16 v[188:189], v227 offset:3136\n"
      "ds_read_b64_tr_b16 v[190:191], v227 offset:3904\n"
      "v_mfma_f32_16x16x32_bf16 v[18:21], v[168:171], a[128:131], 0\n"
      "buffer_load_dwordx4 a[164:167], v255, s[48:51], s75 offen offset:1024 sc1\n"
      "v_mfma_f32_16x16x32_bf16 v[26:29], v[168:171], a[132:135], 0\n"
      "ds_read_b64_tr_b16 v[160:161], v230 offset:128\n"
      "ds_read_b64_tr_b16 v[162:163], v230 offset:896\n"
      "v_mfma_f32_16x16x32_bf16 v[22:25], v[172:175], a[128:131], 0\n"
      "v_mfma_f32_16x16x32_bf16 v[30:33], v[172:175], a[132:135], 0\n"
      "s_waitcnt lgkmcnt(6)\n"
      "ds_read_b64_tr_b16 v[164:165], v227 offset:128\n"
      "ds_read_b64_tr_b16 v[166:167], v227 offset:896\n"
      "v_mfma_f32_16x16x32_bf16 v[96:99], v[176:179], a[128:131], 0\n"
      "buffer_load_dwordx4 a[168:171], v255, s[48:51], s75 offen offset:2048 sc1\n"
      "v_mfma_f32_16x16x32_bf16 v[104:107], v[176:179], a[132:135], 0\n"
      "ds_read_b64_tr_b16 v[168:169], v230 offset:3200\n"
      "ds_read_b64_tr_b16 v[170:171], v230 offset:3968\n"
      "v_mfma_f32_16x16x32_bf16 v[100:103], v[180:183], a[128:131], 0\n"
      "s_cmp_eq_u32 s62, 0\n"
      "s_cselect_b32 s71, 0, 0x40000000\n"
      "v_or_b32 v254, s71, v245\n"
      "v_mfma_f32_16x16x32_bf16 v[108:111], v[180:183], a[132:135], 0\n"
      "s_waitcnt lgkmcnt(6)\n"
      "ds_read_b64_tr_b16 v[172:173], v227 offset:3200\n"
      "ds_read_b64_tr_b16 v[174:175], v227 offset:3968\n"
      "v_mfma_f32_16x16x32_bf16 v[112:115], v[184:187], a[128:131], 0\n"
      "buffer_store_dwordx2 v[246:247], v254, s[32:35], 0 offen sc1\n"
      "v_mfma_f32_16x16x32_bf16 v[120:123], v[184:187], a[132:135], 0\n"
      "ds_read_b64_tr_b16 v[176:177], v230 offset:12288\n"
      "ds_read_b64_tr_b16 v[178:179], v230 offset:13056\n"
      "v_mfma_f32_16x16x32_bf16 v[116:119], v[188:191], a[128:131], 0\n"
      "buffer_load_dwordx4 a[172:175], v255, s[48:51], s75 offen offset:3072 sc1\n"
      "v_mfma_f32_16x16x32_bf16 v[124:127], v[188:191], a[132:135], 0\n"
      "s_waitcnt lgkmcnt(6)\n"
      "ds_read_b64_tr_b16 v[180:181], v227 offset:12288\n"
      "ds_read_b64_tr_b16 v[182:183], v227 offset:13056\n"
      "v_mfma_f32_16x16x32_bf16 v[128:131], v[160:163], a[128:131], 0\n"
      "s_add_u32 s75, s75, 0x1000\n"
      "v_mfma_f32_16x16x32_bf16 v[136:139], v[160:163], a[132:135], 0\n"
      "ds_read_b64_tr_b16 v[184:185], v230 offset:15360\n"
      "ds_read_b64_tr_b16 v[186:187], v230 offset:16128\n"
      "v_mfma_f32_16x16x32_bf16 v[132:135], v[164:167], a[128:131], 0\n"
      "buffer_load_dwordx4 a[176:179], v255, s[48:51], s75 offen sc1\n"
      "v_mfma_f32_16x16x32_bf16 v[140:143], v[164:167], a[132:135], 0\n"
      "s_waitcnt lgkmcnt(6)\n"
      "ds_read_b64_tr_b16 v[188:189], v227 offset:15360\n"
      "ds_read_b64_tr_b16 v[190:191], v227 offset:16128\n"
      "v_mfma_f32_16x16x32_bf16 v[144:147], v[168:171], a[128:131], 0\n"
      "v_mfma_f32_16x16x32_bf16 v[152:155], v[168:171], a[132:135], 0\n"
      "ds_read_b64_tr_b16 v[160:161], v230 offset:12352\n"
      "ds_read_b64_tr_b16 v[162:163], v230 offset:13120\n"
      "v_mfma_f32_16x16x32_bf16 v[148:151], v[172:175], a[128:131], 0\n"
      "buffer_load_dwordx4 a[180:183], v255, s[48:51], s75 offen offset:1024 sc1\n"
      "v_mfma_f32_16x16x32_bf16 v[156:159], v[172:175], a[132:135], 0\n"
      "s_waitcnt vmcnt(11) lgkmcnt(6)\n"
      "ds_read_b64_tr_b16 v[164:165], v227 offset:12352\n"
      "ds_read_b64_tr_b16 v[166:167], v227 offset:13120\n"
      "v_mfma_f32_16x16x32_bf16 v[2:5], v[176:179], a[136:139], v[2:5]\n"
      "v_mfma_f32_16x16x32_bf16 v[10:13], v[176:179], a[140:143], v[10:13]\n"
      "ds_read_b64_tr_b16 v[168:169], v230 offset:15424\n"
      "ds_read_b64_tr_b16 v[170:171], v230 offset:16192\n"
      "v_mfma_f32_16x16x32_bf16 v[6:9], v[180:183], a[136:139], v[6:9]\n"
      "buffer_load_dwordx4 a[184:187], v255, s[48:51], s75 offen offset:2048 sc1\n"
      "v_mfma_f32_16x16x32_bf16 v[14:17], v[180:183], a[140:143], v[14:17]\n"
      "s_waitcnt lgkmcnt(6)\n"
      "ds_read_b64_tr_b16 v[172:173], v227 offset:15424\n"
      "ds_read_b64_tr_b16 v[174:175], v227 offset:16192\n"
      "v_mfma_f32_16x16x32_bf16 v[18:21], v[184:187], a[136:139], v[18:21]\n"
      "v_mfma_f32_16x16x32_bf16 v[26:29], v[184:187], a[140:143], v[26:29]\n"
      "ds_read_b64_tr_b16 v[176:177], v230 offset:12416\n"
      "ds_read_b64_tr_b16 v[178:179], v230 offset:13184\n"
      "v_mfma_f32_16x16x32_bf16 v[22:25], v[188:191], a[136:139], v[22:25]\n"
      "v_mfma_f32_16x16x32_bf16 v[30:33], v[188:191], a[140:143], v[30:33]\n"
      "s_waitcnt lgkmcnt(6)\n"
      "ds_read_b64_tr_b16 v[180:181], v227 offset:12416\n"
      "ds_read_b64_tr_b16 v[182:183], v227 offset:13184\n"
      "v_mfma_f32_16x16x32_bf16 v[96:99], v[160:163], a[136:139], v[96:99]\n"
      "buffer_load_dwordx4 a[188:191], v255, s[48:51], s75 offen offset:3072 sc1\n"
      "v_mfma_f32_16x16x32_bf16 v[104:107], v[160:163], a[140:143], v[104:107]\n"
      "ds_read_b64_tr_b16 v[184:185], v230 offset:15488\n"
      "ds_read_b64_tr_b16 v[186:187], v230 offset:16256\n"
      "v_mfma_f32_16x16x32_bf16 v[100:103], v[164:167], a[136:139], v[100:103]\n"
      "v_mfma_f32_16x16x32_bf16 v[108:111], v[164:167], a[140:143], v[108:111]\n"
      "s_waitcnt lgkmcnt(6)\n"
      "ds_read_b64_tr_b16 v[188:189], v227 offset:15488\n"
      "ds_read_b64_tr_b16 v[190:191], v227 offset:16256\n"
      "v_mfma_f32_16x16x32_bf16 v[112:115], v[168:171], a[136:139], v[112:115]\n"
      "buffer_load_dwordx4 v[192:195], v255, s[48:51], s76 offen sc1\n"
      "v_mfma_f32_16x16x32_bf16 v[120:123], v[168:171], a[140:143], v[120:123]\n"
      "ds_read_b64_tr_b16 v[160:161], v230 offset:24576\n"
      "ds_read_b64_tr_b16 v[162:163], v230 offset:25344\n"
      "v_mfma_f32_16x16x32_bf16 v[116:119], v[172:175], a[136:139], v[116:119]\n"
      "v_mfma_f32_16x16x32_bf16 v[124:127], v[172:175], a[140:143], v[124:127]\n"
      "s_waitcnt lgkmcnt(6)\n"
      "ds_read_b64_tr_b16 v[164:165], v227 offset:24576\n"
      "ds_read_b64_tr_b16 v[166:167], v227 offset:25344\n"
      "v_mfma_f32_16x16x32_bf16 v[128:131], v[176:179], a[136:139], v[128:131]\n"
      "buffer_load_dwordx4 v[196:199], v255, s[48:51], s76 offen offset:1024 sc1\n"
      "v_mfma_f32_16x16x32_bf16 v[136:139], v[176:179], a[140:143], v[136:139]\n"
      "ds_read_b64_tr_b16 v[168:169], v230 offset:27648\n"
      "ds_read_b64_tr_b16 v[170:171], v230 offset:28416\n"
      "v_mfma_f32_16x16x32_bf16 v[132:135], v[180:183], a[136:139], v[132:135]\n"
      "v_mfma_f32_16x16x32_bf16 v[140:143], v[180:183], a[140:143], v[140:143]\n"
      "s_waitcnt lgkmcnt(6)\n"
      "ds_read_b64_tr_b16 v[172:173], v227 offset:27648\n"
      "ds_read_b64_tr_b16 v[174:175], v227 offset:28416\n"
      "v_mfma_f32_16x16x32_bf16 v[144:147], v[184:187], a[136:139], v[144:147]\n"
      "buffer_load_dwordx4 v[200:203], v255, s[48:51], s76 offen offset:2048 sc1\n"
      "v_mfma_f32_16x16x32_bf16 v[152:155], v[184:187], a[140:143], v[152:155]\n"
      "ds_read_b64_tr_b16 v[176:177], v230 offset:24640\n"
      "ds_read_b64_tr_b16 v[178:179], v230 offset:25408\n"
      "v_mfma_f32_16x16x32_bf16 v[148:151], v[188:191], a[136:139], v[148:151]\n"
      "v_mfma_f32_16x16x32_bf16 v[156:159], v[188:191], a[140:143], v[156:159]\n"
      "s_waitcnt vmcnt(14) lgkmcnt(6)\n"
      "ds_read_b64_tr_b16 v[180:181], v227 offset:24640\n"
      "ds_read_b64_tr_b16 v[182:183], v227 offset:25408\n"
      "v_mfma_f32_16x16x32_bf16 v[2:5], v[160:163], a[144:147], v[2:5]\n"
      "s_barrier\n"
      "v_mfma_f32_16x16x32_bf16 v[10:13], v[160:163], a[148:151], v[10:13]\n"
      "ds_read_b64_tr_b16 v[184:185], v230 offset:27712\n"
      "ds_read_b64_tr_b16 v[186:187], v230 offset:28480\n"
      "v_mfma_f32_16x16x32_bf16 v[6:9], v[164:167], a[144:147], v[6:9]\n"
      "buffer_load_dwordx4 v[204:207], v255, s[48:51], s76 offen offset:3072 sc1\n"
      "v_mfma_f32_16x16x32_bf16 v[14:17], v[164:167], a[148:151], v[14:17]\n"
      "s_waitcnt lgkmcnt(6)\n"
      "ds_read_b64_tr_b16 v[188:189], v227 offset:27712\n"
      "ds_read_b64_tr_b16 v[190:191], v227 offset:28480\n"
      "v_mfma_f32_16x16x32_bf16 v[18:21], v[168:171], a[144:147], v[18:21]\n"
      "s_add_u32 m0, s59, 0\n"
      "s_add_u32 s74, s58, 0x102000\n"
      "v_mfma_f32_16x16x32_bf16 v[26:29], v[168:171], a[148:151], v[26:29]\n"
      "ds_read_b64_tr_b16 v[160:161], v230 offset:24704\n"
      "ds_read_b64_tr_b16 v[162:163], v230 offset:25472\n"
      "v_mfma_f32_16x16x32_bf16 v[22:25], v[172:175], a[144:147], v[22:25]\n"
      "buffer_load_dwordx4 v236, s[16:19], s74 offen lds\n"
      "v_mfma_f32_16x16x32_bf16 v[30:33], v[172:175], a[148:151], v[30:33]\n"
      "s_waitcnt lgkmcnt(6)\n"
      "ds_read_b64_tr_b16 v[164:165], v227 offset:24704\n"
      "ds_read_b64_tr_b16 v[166:167], v227 offset:25472\n"
      "v_mfma_f32_16x16x32_bf16 v[96:99], v[176:179], a[144:147], v[96:99]\n"
      "v_mfma_f32_16x16x32_bf16 v[104:107], v[176:179], a[148:151], v[104:107]\n"
      "ds_read_b64_tr_b16 v[168:169], v230 offset:27776\n"
      "ds_read_b64_tr_b16 v[170:171], v230 offset:28544\n"
      "v_mfma_f32_16x16x32_bf16 v[100:103], v[180:183], a[144:147], v[100:103]\n"
      "v_mfma_f32_16x16x32_bf16 v[108:111], v[180:183], a[148:151], v[108:111]\n"
      "s_waitcnt lgkmcnt(6)\n"
      "ds_read_b64_tr_b16 v[172:173], v227 offset:27776\n"
      "ds_read_b64_tr_b16 v[174:175], v227 offset:28544\n"
      "v_mfma_f32_16x16x32_bf16 v[112:115], v[184:187], a[144:147], v[112:115]\n"
      "buffer_load_dwordx4 v237, s[16:19], s74 offen offset:1024 lds\n"
      "v_mfma_f32_16x16x32_bf16 v[120:123], v[184:187], a[148:151], v[120:123]\n"
      "ds_read_b64_tr_b16 v[176:177], v230 offset:36864\n"
      "ds_read_b64_tr_b16 v[178:179], v230 offset:37632\n"
      "v_mfma_f32_16x16x32_bf16 v[116:119], v[188:191], a[144:147], v[116:119]\n"
      "v_mfma_f32_16x16x32_bf16 v[124:127], v[188:191], a[148:151], v[124:127]\n"
      "s_waitcnt lgkmcnt(6)\n"
      "ds_read_b64_tr_b16 v[180:181], v227 offset:36864\n"
      "ds_read_b64_tr_b16 v[182:183], v227 offset:37632\n"
      "v_mfma_f32_16x16x32_bf16 v[128:131], v[160:163], a[144:147], v[128:131]\n"
      "v_mfma_f32_16x16x32_bf16 v[136:139], v[160:163], a[148:151], v[136:139]\n"
      "ds_read_b64_tr_b16 v[184:185], v230 offset:39936\n"
      "ds_read_b64_tr_b16 v[186:187], v230 offset:40704\n"
      "v_mfma_f32_16x16x32_bf16 v[132:135], v[164:167], a[144:147], v[132:135]\n"
      "buffer_load_dwordx4 v238, s[16:19], s74 offen offset:2048 lds\n"
      "v_mfma_f32_16x16x32_bf16 v[140:143], v[164:167], a[148:151], v[140:143]\n"
      "s_waitcnt lgkmcnt(6)\n"
      "ds_read_b64_tr_b16 v[188:189], v227 offset:39936\n"
      "ds_read_b64_tr_b16 v[190:191], v227 offset:40704\n"
      "v_mfma_f32_16x16x32_bf16 v[144:147], v[168:171], a[144:147], v[144:147]\n"
      "s_add_u32 m0, s59, 0xc00\n"
      "v_mfma_f32_16x16x32_bf16 v[152:155], v[168:171], a[148:151], v[152:155]\n"
      "ds_read_b64_tr_b16 v[160:161], v230 offset:36928\n"
      "ds_read_b64_tr_b16 v[162:163], v230 offset:37696\n"
      "v_mfma_f32_16x16x32_bf16 v[148:151], v[172:175], a[144:147], v[148:151]\n"
      "v_mfma_f32_16x16x32_bf16 v[156:159], v[172:175], a[148:151], v[156:159]\n"
      "s_waitcnt vmcnt(16) lgkmcnt(6)\n"
      "ds_read_b64_tr_b16 v[164:165], v227 offset:36928\n"
      "ds_read_b64_tr_b16 v[166:167], v227 offset:37696\n"
      "v_mfma_f32_16x16x32_bf16 v[2:5], v[176:179], a[152:155], v[2:5]\n"
      "buffer_load_dwordx4 v236, s[20:23], s74 offen lds\n"
      "v_mfma_f32_16x16x32_bf16 v[10:13], v[176:179], a[156:159], v[10:13]\n"
      "ds_read_b64_tr_b16 v[168:169], v230 offset:40000\n"
      "ds_read_b64_tr_b16 v[170:171], v230 offset:40768\n"
      "v_mfma_f32_16x16x32_bf16 v[6:9], v[180:183], a[152:155], v[6:9]\n"
      "v_mfma_f32_16x16x32_bf16 v[14:17], v[180:183], a[156:159], v[14:17]\n"
      "s_waitcnt lgkmcnt(6)\n"
      "ds_read_b64_tr_b16 v[172:173], v227 offset:40000\n"
      "ds_read_b64_tr_b16 v[174:175], v227 offset:40768\n"
      "v_mfma_f32_16x16x32_bf16 v[18:21], v[184:187], a[152:155], v[18:21]\n"
      "v_mfma_f32_16x16x32_bf16 v[26:29], v[184:187], a[156:159], v[26:29]\n"
      "ds_read_b64_tr_b16 v[176:177], v230 offset:36992\n"
      "ds_read_b64_tr_b16 v[178:179], v230 offset:37760\n"
      "v_mfma_f32_16x16x32_bf16 v[22:25], v[188:191], a[152:155], v[22:25]\n"
      "buffer_load_dwordx4 v237, s[20:23], s74 offen offset:1024 lds\n"
      "v_mfma_f32_16x16x32_bf16 v[30:33], v[188:191], a[156:159], v[30:33]\n"
      "s_waitcnt lgkmcnt(6)\n"
      "ds_read_b64_tr_b16 v[180:181], v227 offset:36992\n"
      "ds_read_b64_tr_b16 v[182:183], v227 offset:37760\n"
      "v_mfma_f32_16x16x32_bf16 v[96:99], v[160:163], a[152:155], v[96:99]\n"
      "v_mfma_f32_16x16x32_bf16 v[104:107], v[160:163], a[156:159], v[104:107]\n"
      "ds_read_b64_tr_b16 v[184:185], v230 offset:40064\n"
      "ds_read_b64_tr_b16 v[186:187], v230 offset:40832\n"
      "v_mfma_f32_16x16x32_bf16 v[100:103], v[164:167], a[152:155], v[100:103]\n"
      "v_mfma_f32_16x16x32_bf16 v[108:111], v[164:167], a[156:159], v[108:111]\n"
      "s_waitcnt lgkmcnt(6)\n"
      "ds_read_b64_tr_b16 v[188:189], v227 offset:40064\n"
      "ds_read_b64_tr_b16 v[190:191], v227 offset:40832\n"
      "v_mfma_f32_16x16x32_bf16 v[112:115], v[168:171], a[152:155], v[112:115]\n"
      "buffer_load_dwordx4 v238, s[20:23], s74 offen offset:2048 lds\n"
      "v_mfma_f32_16x16x32_bf16 v[120:123], v[168:171], a[156:159], v[120:123]\n"
      "s_add_u32 s75, s75, 0x3000\n"
      "ds_read_b64_tr_b16 v[160:161], v231\n"
      "ds_read_b64_tr_b16 v[162:163], v231 offset:768\n"
      "v_mfma_f32_16x16x32_bf16 v[116:119], v[172:175], a[152:155], v[116:119]\n"
      "v_mfma_f32_16x16x32_bf16 v[124:127], v[172:175], a[156:159], v[124:127]\n"
      "s_waitcnt lgkmcnt(6)\n"
      "ds_read_b64_tr_b16 v[164:165], v228\n"
      "ds_read_b64_tr_b16 v[166:167], v228 offset:768\n"
      "v_mfma_f32_16x16x32_bf16 v[128:131], v[176:179], a[152:155], v[128:131]\n"
      "v_mfma_f32_16x16x32_bf16 v[136:139], v[176:179], a[156:159], v[136:139]\n"
      "ds_read_b64_tr_b16 v[168:169], v231 offset:3072\n"
      "ds_read_b64_tr_b16 v[170:171], v231 offset:3840\n"
      "v_mfma_f32_16x16x32_bf16 v[132:135], v[180:183], a[152:155], v[132:135]\n"
      "buffer_load_dwordx4 a[192:195], v255, s[48:51], s75 offen sc1\n"
      "v_mfma_f32_16x16x32_bf16 v[140:143], v[180:183], a[156:159], v[140:143]\n"
      "s_waitcnt lgkmcnt(6)\n"
      "ds_read_b64_tr_b16 v[172:173], v228 offset:3072\n"
      "ds_read_b64_tr_b16 v[174:175], v228 offset:3840\n"
      "v_mfma_f32_16x16x32_bf16 v[144:147], v[184:187], a[152:155], v[144:147]\n"
      "v_mfma_f32_16x16x32_bf16 v[152:155], v[184:187], a[156:159], v[152:155]\n"
      "ds_read_b64_tr_b16 v[176:177], v231 offset:64\n"
      "ds_read_b64_tr_b16 v[178:179], v231 offset:832\n"
      "v_mfma_f32_16x16x32_bf16 v[148:151], v[188:191], a[152:155], v[148:151]\n"
      "buffer_load_dwordx4 a[196:199], v255, s[48:51], s75 offen offset:1024 sc1\n"
      "v_mfma_f32_16x16x32_bf16 v[156:159], v[188:191], a[156:159], v[156:159]\n"
      "s_waitcnt vmcnt(19) lgkmcnt(6)\n"
      "ds_read_b64_tr_b16 v[180:181], v228 offset:64\n"
      "ds_read_b64_tr_b16 v[182:183], v228 offset:832\n"
      "v_mfma_f32_16x16x32_bf16 v[2:5], v[160:163], a[160:163], v[2:5]\n"
      "s_barrier\n"
      "v_mfma_f32_16x16x32_bf16 v[10:13], v[160:163], a[164:167], v[10:13]\n"
      "ds_read_b64_tr_b16 v[184:185], v231 offset:3136\n"
      "ds_read_b64_tr_b16 v[186:187], v231 offset:3904\n"
      "v_mfma_f32_16x16x32_bf16 v[6:9], v[164:167], a[160:163], v[6:9]\n"
      "buffer_load_dwordx4 a[200:203], v255, s[48:51], s75 offen offset:2048 sc1\n"
      "v_mfma_f32_16x16x32_bf16 v[14:17], v[164:167], a[164:167], v[14:17]\n"
      "s_waitcnt lgkmcnt(6)\n"
      "ds_read_b64_tr_b16 v[188:189], v228 offset:3136\n"
      "ds_read_b64_tr_b16 v[190:191], v228 offset:3904\n"
      "v_mfma_f32_16x16x32_bf16 v[18:21], v[168:171], a[160:163], v[18:21]\n"
      "s_add_u32 m0, s59, 0x6000\n"
      "s_add_u32 s74, s58, 0x12d000\n"
      "v_mfma_f32_16x16x32_bf16 v[26:29], v[168:171], a[164:167], v[26:29]\n"
      "ds_read_b64_tr_b16 v[160:161], v231 offset:128\n"
      "ds_read_b64_tr_b16 v[162:163], v231 offset:896\n"
      "v_mfma_f32_16x16x32_bf16 v[22:25], v[172:175], a[160:163], v[22:25]\n"
      "s_waitcnt vmcnt(9)\n"
      "v_add_f32 v192, v192, v196\n"
      "v_add_f32 v193, v193, v197\n"
      "v_mfma_f32_16x16x32_bf16 v[30:33], v[172:175], a[164:167], v[30:33]\n"
      "s_waitcnt lgkmcnt(6)\n"
      "ds_read_b64_tr_b16 v[164:165], v228 offset:128\n"
      "ds_read_b64_tr_b16 v[166:167], v228 offset:896\n"
      "v_mfma_f32_16x16x32_bf16 v[96:99], v[176:179], a[160:163], v[96:99]\n"
      "buffer_load_dwordx4 v236, s[16:19], s74 offen lds\n"
      "v_mfma_f32_16x16x32_bf16 v[104:107], v[176:179], a[164:167], v[104:107]\n"
      "ds_read_b64_tr_b16 v[168:169], v231 offset:3200\n"
      "ds_read_b64_tr_b16 v[170:171], v231 offset:3968\n"
      "v_mfma_f32_16x16x32_bf16 v[100:103], v[180:183], a[160:163], v[100:103]\n"
      "v_add_f32 v194, v194, v198\n"
      "v_add_f32 v195, v195, v199\n"
      "v_mfma_f32_16x16x32_bf16 v[108:111], v[180:183], a[164:167], v[108:111]\n"
      "s_waitcnt lgkmcnt(6)\n"
      "ds_read_b64_tr_b16 v[172:173], v228 offset:3200\n"
      "ds_read_b64_tr_b16 v[174:175], v228 offset:3968\n"
      "v_mfma_f32_16x16x32_bf16 v[112:115], v[184:187], a[160:163], v[112:115]\n"
      "v_add_f32 v192, v192, v200\n"
      "v_add_f32 v193, v193, v201\n"
      "v_mfma_f32_16x16x32_bf16 v[120:123], v[184:187], a[164:167], v[120:123]\n"
      "ds_read_b64_tr_b16 v[176:177], v231 offset:12288\n"
      "ds_read_b64_tr_b16 v[178:179], v231 offset:13056\n"
      "v_mfma_f32_16x16x32_bf16 v[116:119], v[188:191], a[160:163], v[116:119]\n"
      "buffer_load_dwordx4 v237, s[16:19], s74 offen offset:1024 lds\n"
      "v_mfma_f32_16x16x32_bf16 v[124:127], v[188:191], a[164:167], v[124:127]\n"
      "s_waitcnt lgkmcnt(6)\n"
      "ds_read_b64_tr_b16 v[180:181], v228 offset:12288\n"
      "ds_read_b64_tr_b16 v[182:183], v228 offset:13056\n"
      "v_mfma_f32_16x16x32_bf16 v[128:131], v[160:163], a[160:163], v[128:131]\n"
      "v_add_f32 v194, v194, v202\n"
      "v_add_f32 v195, v195, v203\n"
      "v_mfma_f32_16x16x32_bf16 v[136:139], v[160:163], a[164:167], v[136:139]\n"
      "ds_read_b64_tr_b16 v[184:185], v231 offset:15360\n"
      "ds_read_b64_tr_b16 v[186:187], v231 offset:16128\n"
      "v_mfma_f32_16x16x32_bf16 v[132:135], v[164:167], a[160:163], v[132:135]\n"
      "v_add_f32 v192, v192, v204\n"
      "v_add_f32 v193, v193, v205\n"
      "v_mfma_f32_16x16x32_bf16 v[140:143], v[164:167], a[164:167], v[140:143]\n"
      "s_waitcnt lgkmcnt(6)\n"
      "ds_read_b64_tr_b16 v[188:189], v228 offset:15360\n"
      "ds_read_b64_tr_b16 v[190:191], v228 offset:16128\n"
      "v_mfma_f32_16x16x32_bf16 v[144:147], v[168:171], a[160:163], v[144:147]\n"
      "buffer_load_dwordx4 v238, s[16:19], s74 offen offset:2048 lds\n"
      "v_mfma_f32_16x16x32_bf16 v[152:155], v[168:171], a[164:167], v[152:155]\n"
      "s_add_u32 m0, s59, 0x6c00\n"
      "ds_read_b64_tr_b16 v[160:161], v231 offset:12352\n"
      "ds_read_b64_tr_b16 v[162:163], v231 offset:13120\n"
      "v_mfma_f32_16x16x32_bf16 v[148:151], v[172:175], a[160:163], v[148:151]\n"
      "v_add_f32 v194, v194, v206\n"
      "v_add_f32 v195, v195, v207\n"
      "v_mfma_f32_16x16x32_bf16 v[156:159], v[172:175], a[164:167], v[156:159]\n"
      "s_waitcnt lgkmcnt(6)\n"
      "ds_read_b64_tr_b16 v[164:165], v228 offset:12352\n"
      "ds_read_b64_tr_b16 v[166:167], v228 offset:13120\n"
      "v_mfma_f32_16x16x32_bf16 v[2:5], v[176:179], a[168:171], v[2:5]\n"
      "v_mov_b32 v196, v192\n"
      "v_mov_b32 v197, v193\n"
      "v_mfma_f32_16x16x32_bf16 v[10:13], v[176:179], a[172:175], v[10:13]\n"
      "ds_read_b64_tr_b16 v[168:169], v231 offset:15424\n"
      "ds_read_b64_tr_b16 v[170:171], v231 offset:16192\n"
      "v_mfma_f32_16x16x32_bf16 v[6:9], v[180:183], a[168:171], v[6:9]\n"
      "buffer_load_dwordx4 v236, s[20:23], s74 offen lds\n"
      "v_mfma_f32_16x16x32_bf16 v[14:17], v[180:183], a[172:175], v[14:17]\n"
      "s_waitcnt lgkmcnt(6)\n"
      "ds_read_b64_tr_b16 v[172:173], v228 offset:15424\n"
      "ds_read_b64_tr_b16 v[174:175], v228 offset:16192\n"
      "v_mfma_f32_16x16x32_bf16 v[18:21], v[184:187], a[168:171], v[18:21]\n"
      "v_mov_b32 v198, v194\n"
      "v_mov_b32 v199, v195\n"
      "v_mfma_f32_16x16x32_bf16 v[26:29], v[184:187], a[172:175], v[26:29]\n"
      "ds_read_b64_tr_b16 v[176:177], v231 offset:12416\n"
      "ds_read_b64_tr_b16 v[178:179], v231 offset:13184\n"
      "v_mfma_f32_16x16x32_bf16 v[22:25], v[188:191], a[168:171], v[22:25]\n"
      "v_permlane32_swap_b32 v192, v196\n"
      "v_mfma_f32_16x16x32_bf16 v[30:33], v[188:191], a[172:175], v[30:33]\n"
      "s_waitcnt lgkmcnt(6)\n"
      "ds_read_b64_tr_b16 v[180:181], v228 offset:12416\n"
      "ds_read_b64_tr_b16 v[182:183], v228 offset:13184\n"
      "v_mfma_f32_16x16x32_bf16 v[96:99], v[160:163], a[168:171], v[96:99]\n"
      "buffer_load_dwordx4 v237, s[20:23], s74 offen offset:1024 lds\n"
      "v_mfma_f32_16x16x32_bf16 v[104:107], v[160:163], a[172:175], v[104:107]\n"
      "ds_read_b64_tr_b16 v[184:185], v231 offset:15488\n"
      "ds_read_b64_tr_b16 v[186:187], v231 offset:16256\n"
      "v_mfma_f32_16x16x32_bf16 v[100:103], v[164:167], a[168:171], v[100:103]\n"
      "v_permlane32_swap_b32 v193, v197\n"
      "v_mfma_f32_16x16x32_bf16 v[108:111], v[164:167], a[172:175], v[108:111]\n"
      "s_waitcnt lgkmcnt(6)\n"
      "ds_read_b64_tr_b16 v[188:189], v228 offset:15488\n"
      "ds_read_b64_tr_b16 v[190:191], v228 offset:16256\n"
      "v_mfma_f32_16x16x32_bf16 v[112:115], v[168:171], a[168:171], v[112:115]\n"
      "s_waitcnt vmcnt(8)\n"
      "ds_read_b64_tr_b16 v[48:49], v224\n"
      "v_permlane32_swap_b32 v194, v198\n"
      "v_mfma_f32_16x16x32_bf16 v[120:123], v[168:171], a[172:175], v[120:123]\n"
      "ds_read_b64_tr_b16 v[160:161], v231 offset:24576\n"
      "ds_read_b64_tr_b16 v[162:163], v231 offset:25344\n"
      "v_mfma_f32_16x16x32_bf16 v[116:119], v[172:175], a[168:171], v[116:119]\n"
      "buffer_load_dwordx4 v238, s[20:23], s74 offen offset:2048 lds\n"
      "v_mfma_f32_16x16x32_bf16 v[124:127], v[172:175], a[172:175], v[124:127]\n"
      "s_waitcnt lgkmcnt(7)\n"
      "ds_read_b64_tr_b16 v[164:165], v228 offset:24576\n"
      "ds_read_b64_tr_b16 v[166:167], v228 offset:25344\n"
      "v_mfma_f32_16x16x32_bf16 v[128:131], v[176:179], a[168:171], v[128:131]\n"
      "ds_read_b64_tr_b16 v[50:51], v224 offset:768\n"
      "v_permlane32_swap_b32 v195, v199\n"
      "v_mfma_f32_16x16x32_bf16 v[136:139], v[176:179], a[172:175], v[136:139]\n"
      "ds_read_b64_tr_b16 v[168:169], v231 offset:27648\n"
      "ds_read_b64_tr_b16 v[170:171], v231 offset:28416\n"
      "v_mfma_f32_16x16x32_bf16 v[132:135], v[180:183], a[168:171], v[132:135]\n"
      "ds_read_b64_tr_b16 v[52:53], v224 offset:3072\n"
      "ds_read_b64_tr_b16 v[54:55], v224 offset:3840\n"
      "v_mfma_f32_16x16x32_bf16 v[140:143], v[180:183], a[172:175], v[140:143]\n"
      "s_waitcnt lgkmcnt(10)\n"
      "ds_read_b64_tr_b16 v[172:173], v228 offset:27648\n"
      "ds_read_b64_tr_b16 v[174:175], v228 offset:28416\n"
      "v_mfma_f32_16x16x32_bf16 v[144:147], v[184:187], a[168:171], v[144:147]\n"
      "buffer_load_dwordx4 a[204:207], v255, s[48:51], s75 offen offset:3072 sc1\n"
      "v_mfma_f32_16x16x32_bf16 v[152:155], v[184:187], a[172:175], v[152:155]\n"
      "s_add_u32 s75, s75, 0x1000\n"
      "ds_read_b64_tr_b16 v[176:177], v231 offset:24640\n"
      "ds_read_b64_tr_b16 v[178:179], v231 offset:25408\n"
      "v_mfma_f32_16x16x32_bf16 v[148:151], v[188:191], a[168:171], v[148:151]\n"
      "ds_read_b64_tr_b16 v[56:57], v224 offset:64\n"
      "s_waitcnt lgkmcnt(7)\n"
      "ds_read_b64_tr_b16 v[58:59], v224 offset:832\n"
      "v_mfma_f32_16x16x32_bf16 v[156:159], v[188:191], a[172:175], v[156:159]\n"
      "ds_read_b64_tr_b16 v[180:181], v228 offset:24640\n"
      "ds_read_b64_tr_b16 v[182:183], v228 offset:25408\n"
      "v_mfma_f32_16x16x32_bf16 v[2:5], v[160:163], a[176:179], v[2:5]\n"
      "buffer_load_dwordx4 a[208:211], v255, s[48:51], s75 offen sc1\n"
      "v_mfma_f32_16x16x32_bf16 v[10:13], v[160:163], a[180:183], v[10:13]\n"
      "ds_read_b64_tr_b16 v[184:185], v231 offset:27712\n"
      "ds_read_b64_tr_b16 v[186:187], v231 offset:28480\n"
      "v_mfma_f32_16x16x32_bf16 v[6:9], v[164:167], a[176:179], v[6:9]\n"
      "ds_read_b64_tr_b16 v[60:61], v224 offset:3136\n"
      "ds_read_b64_tr_b16 v[62:63], v224 offset:3904\n"
      "v_mfma_f32_16x16x32_bf16 v[14:17], v[164:167], a[180:183], v[14:17]\n"
      "s_waitcnt lgkmcnt(10)\n"
      "ds_read_b64_tr_b16 v[188:189], v228 offset:27712\n"
      "ds_read_b64_tr_b16 v[190:191], v228 offset:28480\n"
      "v_mfma_f32_16x16x32_bf16 v[18:21], v[168:171], a[176:179], v[18:21]\n"
      "buffer_load_dwordx4 a[212:215], v255, s[48:51], s75 offen offset:1024 sc1\n"
      "v_mfma_f32_16x16x32_bf16 v[26:29], v[168:171], a[180:183], v[26:29]\n"
      "ds_read_b64_tr_b16 v[160:161], v231 offset:24704\n"
      "ds_read_b64_tr_b16 v[162:163], v231 offset:25472\n"
      "v_mfma_f32_16x16x32_bf16 v[22:25], v[172:175], a[176:179], v[22:25]\n"
      "ds_read_b64_tr_b16 v[64:65], v224 offset:128\n"
      "s_waitcnt lgkmcnt(7)\n"
      "ds_read_b64_tr_b16 v[66:67], v224 offset:896\n"
      "v_mfma_f32_16x16x32_bf16 v[30:33], v[172:175], a[180:183], v[30:33]\n"
      "ds_read_b64_tr_b16 v[164:165], v228 offset:24704\n"
      "ds_read_b64_tr_b16 v[166:167], v228 offset:25472\n"
      "v_mfma_f32_16x16x32_bf16 v[96:99], v[176:179], a[176:179], v[96:99]\n"
      "buffer_load_dwordx4 a[216:219], v255, s[48:51], s75 offen offset:2048 sc1\n"
      "v_mfma_f32_16x16x32_bf16 v[104:107], v[176:179], a[180:183], v[104:107]\n"
      "ds_read_b64_tr_b16 v[168:169], v231 offset:27776\n"
      "ds_read_b64_tr_b16 v[170:171], v231 offset:28544\n"
      "v_mfma_f32_16x16x32_bf16 v[100:103], v[180:183], a[176:179], v[100:103]\n"
      "ds_read_b64_tr_b16 v[68:69], v224 offset:3200\n"
      "ds_read_b64_tr_b16 v[70:71], v224 offset:3968\n"
      "v_mfma_f32_16x16x32_bf16 v[108:111], v[180:183], a[180:183], v[108:111]\n"
      "s_waitcnt lgkmcnt(10)\n"
      "ds_read_b64_tr_b16 v[172:173], v228 offset:27776\n"
      "ds_read_b64_tr_b16 v[174:175], v228 offset:28544\n"
      "v_mfma_f32_16x16x32_bf16 v[112:115], v[184:187], a[176:179], v[112:115]\n"
      "v_add_f32 v192, v192, v196\n"
      "v_add_f32 v193, v193, v197\n"
      "v_mfma_f32_16x16x32_bf16 v[120:123], v[184:187], a[180:183], v[120:123]\n"
      "ds_read_b64_tr_b16 v[176:177], v231 offset:36864\n"
      "ds_read_b64_tr_b16 v[178:179], v231 offset:37632\n"
      "v_mfma_f32_16x16x32_bf16 v[116:119], v[188:191], a[176:179], v[116:119]\n"
      "buffer_load_dwordx4 a[220:223], v255, s[48:51], s75 offen offset:3072 sc1\n"
      "v_mfma_f32_16x16x32_bf16 v[124:127], v[188:191], a[180:183], v[124:127]\n"
      "s_waitcnt lgkmcnt(8)\n"
      "ds_read_b64_tr_b16 v[180:181], v228 offset:36864\n"
      "ds_read_b64_tr_b16 v[182:183], v228 offset:37632\n"
      "v_mfma_f32_16x16x32_bf16 v[128:131], v[160:163], a[176:179], v[128:131]\n"
      "s_add_u32 s75, s75, 0x3000\n"
      "v_add_f32 v194, v194, v198\n"
      "v_add_f32 v195, v195, v199\n"
      "v_mfma_f32_16x16x32_bf16 v[136:139], v[160:163], a[180:183], v[136:139]\n"
      "ds_read_b64_tr_b16 v[184:185], v231 offset:39936\n"
      "ds_read_b64_tr_b16 v[186:187], v231 offset:40704\n"
      "v_mfma_f32_16x16x32_bf16 v[132:135], v[164:167], a[176:179], v[132:135]\n"
      "buffer_load_dwordx4 a[224:227], v255, s[48:51], s75 offen sc1\n"
      "v_mfma_f32_16x16x32_bf16 v[140:143], v[164:167], a[180:183], v[140:143]\n"
      "s_waitcnt lgkmcnt(6)\n"
      "ds_read_b64_tr_b16 v[188:189], v228 offset:39936\n"
      "ds_read_b64_tr_b16 v[190:191], v228 offset:40704\n"
      "v_mfma_f32_16x16x32_bf16 v[144:147], v[168:171], a[176:179], v[144:147]\n"
      "v_mov_b32 v196, v192\n"
      "v_mov_b32 v197, v193\n"
      "v_mfma_f32_16x16x32_bf16 v[152:155], v[168:171], a[180:183], v[152:155]\n"
      "ds_read_b64_tr_b16 v[160:161], v231 offset:36928\n"
      "ds_read_b64_tr_b16 v[162:163], v231 offset:37696\n"
      "v_mfma_f32_16x16x32_bf16 v[148:151], v[172:175], a[176:179], v[148:151]\n"
      "buffer_load_dwordx4 a[228:231], v255, s[48:51], s75 offen offset:1024 sc1\n"
      "v_mfma_f32_16x16x32_bf16 v[156:159], v[172:175], a[180:183], v[156:159]\n"
      "s_waitcnt lgkmcnt(6)\n"
      "ds_read_b64_tr_b16 v[164:165], v228 offset:36928\n"
      "ds_read_b64_tr_b16 v[166:167], v228 offset:37696\n"
      "v_mfma_f32_16x16x32_bf16 v[2:5], v[176:179], a[184:187], v[2:5]\n"
      "v_mov_b32 v198, v194\n"
      "v_mov_b32 v199, v195\n"
      "v_mfma_f32_16x16x32_bf16 v[10:13], v[176:179], a[188:191], v[10:13]\n"
      "ds_read_b64_tr_b16 v[168:169], v231 offset:40000\n"
      "ds_read_b64_tr_b16 v[170:171], v231 offset:40768\n"
      "v_mfma_f32_16x16x32_bf16 v[6:9], v[180:183], a[184:187], v[6:9]\n"
      "buffer_load_dwordx4 a[232:235], v255, s[48:51], s75 offen offset:2048 sc1\n"
      "v_mfma_f32_16x16x32_bf16 v[14:17], v[180:183], a[188:191], v[14:17]\n"
      "s_waitcnt lgkmcnt(6)\n"
      "ds_read_b64_tr_b16 v[172:173], v228 offset:40000\n"
      "ds_read_b64_tr_b16 v[174:175], v228 offset:40768\n"
      "v_mfma_f32_16x16x32_bf16 v[18:21], v[184:187], a[184:187], v[18:21]\n"
      "v_permlane16_swap_b32 v192, v196\n"
      "v_mfma_f32_16x16x32_bf16 v[26:29], v[184:187], a[188:191], v[26:29]\n"
      "ds_read_b64_tr_b16 v[176:177], v231 offset:36992\n"
      "ds_read_b64_tr_b16 v[178:179], v231 offset:37760\n"
      "v_mfma_f32_16x16x32_bf16 v[22:25], v[188:191], a[184:187], v[22:25]\n"
      "v_permlane16_swap_b32 v193, v197\n"
      "v_mfma_f32_16x16x32_bf16 v[30:33], v[188:191], a[188:191], v[30:33]\n"
      "s_waitcnt lgkmcnt(6)\n"
      "ds_read_b64_tr_b16 v[180:181], v228 offset:36992\n"
      "ds_read_b64_tr_b16 v[182:183], v228 offset:37760\n"
      "v_mfma_f32_16x16x32_bf16 v[96:99], v[160:163], a[184:187], v[96:99]\n"
      "buffer_load_dwordx4 a[236:239], v255, s[48:51], s75 offen offset:3072 sc1\n"
      "v_mfma_f32_16x16x32_bf16 v[104:107], v[160:163], a[188:191], v[104:107]\n"
      "s_add_u32 s75, s75, 0x1000\n"
      "ds_read_b64_tr_b16 v[184:185], v231 offset:40064\n"
      "ds_read_b64_tr_b16 v[186:187], v231 offset:40832\n"
      "v_mfma_f32_16x16x32_bf16 v[100:103], v[164:167], a[184:187], v[100:103]\n"
      "v_permlane16_swap_b32 v194, v198\n"
      "v_mfma_f32_16x16x32_bf16 v[108:111], v[164:167], a[188:191], v[108:111]\n"
      "s_waitcnt lgkmcnt(6)\n"
      "ds_read_b64_tr_b16 v[188:189], v228 offset:40064\n"
      "ds_read_b64_tr_b16 v[190:191], v228 offset:40832\n"
      "v_mfma_f32_16x16x32_bf16 v[112:115], v[168:171], a[184:187], v[112:115]\n"
      "buffer_load_dwordx4 a[240:243], v255, s[48:51], s75 offen sc1\n"
      "v_mfma_f32_16x16x32_bf16 v[120:123], v[168:171], a[188:191], v[120:123]\n"
      "ds_read_b64_tr_b16 v[160:161], v232\n"
      "ds_read_b64_tr_b16 v[162:163], v232 offset:768\n"
      "v_mfma_f32_16x16x32_bf16 v[116:119], v[172:175], a[184:187], v[116:119]\n"
      "s_waitcnt vmcnt(10)\n"
      "ds_read_b64_tr_b16 v[72:73], v224 offset:24576\n"
      "ds_read_b64_tr_b16 v[74:75], v224 offset:25344\n"
      "v_mfma_f32_16x16x32_bf16 v[124:127], v[172:175], a[188:191], v[124:127]\n"
      "s_waitcnt lgkmcnt(8)\n"
      "ds_read_b64_tr_b16 v[164:165], v229\n"
      "ds_read_b64_tr_b16 v[166:167], v229 offset:768\n"
      "v_mfma_f32_16x16x32_bf16 v[128:131], v[176:179], a[184:187], v[128:131]\n"
      "buffer_load_dwordx4 a[244:247], v255, s[48:51], s75 offen offset:1024 sc1\n"
      "v_mfma_f32_16x16x32_bf16 v[136:139], v[176:179], a[188:191], v[136:139]\n"
      "ds_read_b64_tr_b16 v[168:169], v232 offset:3072\n"
      "ds_read_b64_tr_b16 v[170:171], v232 offset:3840\n"
      "v_mfma_f32_16x16x32_bf16 v[132:135], v[180:183], a[184:187], v[132:135]\n"
      "ds_read_b64_tr_b16 v[76:77], v224 offset:27648\n"
      "ds_read_b64_tr_b16 v[78:79], v224 offset:28416\n"
      "v_mfma_f32_16x16x32_bf16 v[140:143], v[180:183], a[188:191], v[140:143]\n"
      "s_waitcnt lgkmcnt(10)\n"
      "ds_read_b64_tr_b16 v[172:173], v229 offset:3072\n"
      "ds_read_b64_tr_b16 v[174:175], v229 offset:3840\n"
      "v_mfma_f32_16x16x32_bf16 v[144:147], v[184:187], a[184:187], v[144:147]\n"
      "buffer_load_dwordx4 a[248:251], v255, s[48:51], s75 offen offset:2048 sc1\n"
      "v_mfma_f32_16x16x32_bf16 v[152:155], v[184:187], a[188:191], v[152:155]\n"
      "ds_read_b64_tr_b16 v[176:177], v232 offset:64\n"
      "ds_read_b64_tr_b16 v[178:179], v232 offset:832\n"
      "v_mfma_f32_16x16x32_bf16 v[148:151], v[188:191], a[184:187], v[148:151]\n"
      "ds_read_b64_tr_b16 v[80:81], v224 offset:24640\n"
      "s_waitcnt lgkmcnt(7)\n"
      "ds_read_b64_tr_b16 v[82:83], v224 offset:25408\n"
      "v_mfma_f32_16x16x32_bf16 v[156:159], v[188:191], a[188:191], v[156:159]\n"
      "ds_read_b64_tr_b16 v[180:181], v229 offset:64\n"
      "ds_read_b64_tr_b16 v[182:183], v229 offset:832\n"
      "v_mfma_f32_16x16x32_bf16 v[2:5], v[160:163], a[192:195], v[2:5]\n"
      "s_barrier\n"
      "ds_read_b64_tr_b16 v[84:85], v224 offset:27712\n"
      "v_permlane16_swap_b32 v195, v199\n"
      "v_mfma_f32_16x16x32_bf16 v[10:13], v[160:163], a[196:199], v[10:13]\n"
      "ds_read_b64_tr_b16 v[184:185], v232 offset:3136\n"
      "ds_read_b64_tr_b16 v[186:187], v232 offset:3904\n"
      "v_mfma_f32_16x16x32_bf16 v[6:9], v[164:167], a[192:195], v[6:9]\n"
      "buffer_load_dwordx4 a[252:255], v255, s[48:51], s75 offen offset:3072 sc1\n"
      "v_mfma_f32_16x16x32_bf16 v[14:17], v[164:167], a[196:199], v[14:17]\n"
      "s_waitcnt lgkmcnt(9)\n"
      "ds_read_b64_tr_b16 v[188:189], v229 offset:3136\n"
      "ds_read_b64_tr_b16 v[190:191], v229 offset:3904\n"
      "v_mfma_f32_16x16x32_bf16 v[18:21], v[168:171], a[192:195], v[18:21]\n"
      "s_add_u32 m0, s69, 0x24000\n"
      "s_add_u32 s74, s80, 0\n"
      "ds_read_b64_tr_b16 v[86:87], v224 offset:28480\n"
      "v_mfma_f32_16x16x32_bf16 v[26:29], v[168:171], a[196:199], v[26:29]\n"
      "ds_read_b64_tr_b16 v[160:161], v232 offset:128\n"
      "ds_read_b64_tr_b16 v[162:163], v232 offset:896\n"
      "v_mfma_f32_16x16x32_bf16 v[22:25], v[172:175], a[192:195], v[22:25]\n"
      "buffer_load_dwordx4 v218, s[24:27], s74 offen lds\n"
      "v_mfma_f32_16x16x32_bf16 v[30:33], v[172:175], a[196:199], v[30:33]\n"
      "s_waitcnt lgkmcnt(8)\n"
      "ds_read_b64_tr_b16 v[164:165], v229 offset:128\n"
      "ds_read_b64_tr_b16 v[166:167], v229 offset:896\n"
      "v_mfma_f32_16x16x32_bf16 v[96:99], v[176:179], a[192:195], v[96:99]\n"
      "ds_read_b64_tr_b16 v[88:89], v224 offset:24704\n"
      "v_add_f32 v192, v192, v196\n"
      "v_mfma_f32_16x16x32_bf16 v[104:107], v[176:179], a[196:199], v[104:107]\n"
      "ds_read_b64_tr_b16 v[168:169], v232 offset:3200\n"
      "ds_read_b64_tr_b16 v[170:171], v232 offset:3968\n"
      "v_mfma_f32_16x16x32_bf16 v[100:103], v[180:183], a[192:195], v[100:103]\n"
      "buffer_load_dwordx4 v219, s[24:27], s74 offen offset:1024 lds\n"
      "v_mfma_f32_16x16x32_bf16 v[108:111], v[180:183], a[196:199], v[108:111]\n"
      "s_waitcnt lgkmcnt(8)\n"
      "ds_read_b64_tr_b16 v[172:173], v229 offset:3200\n"
      "ds_read_b64_tr_b16 v[174:175], v229 offset:3968\n"
      "v_mfma_f32_16x16x32_bf16 v[112:115], v[184:187], a[192:195], v[112:115]\n"
      "s_add_u32 m0, s59, 0xc000\n"
      "s_add_u32 s74, s80, 0x4000\n"
      "ds_read_b64_tr_b16 v[90:91], v224 offset:25472\n"
      "v_mfma_f32_16x16x32_bf16 v[120:123], v[184:187], a[196:199], v[120:123]\n"
      "ds_read_b64_tr_b16 v[176:177], v232 offset:12288\n"
      "ds_read_b64_tr_b16 v[178:179], v232 offset:13056\n"
      "v_mfma_f32_16x16x32_bf16 v[116:119], v[188:191], a[192:195], v[116:119]\n"
      "ds_read_b64_tr_b16 v[92:93], v224 offset:27776\n"
      "ds_read_b64_tr_b16 v[94:95], v224 offset:28544\n"
      "v_mfma_f32_16x16x32_bf16 v[124:127], v[188:191], a[196:199], v[124:127]\n"
      "s_waitcnt lgkmcnt(10)\n"
      "ds_read_b64_tr_b16 v[180:181], v229 offset:12288\n"
      "ds_read_b64_tr_b16 v[182:183], v229 offset:13056\n"
      "v_mfma_f32_16x16x32_bf16 v[128:131], v[160:163], a[192:195], v[128:131]\n"
      "buffer_load_dwordx4 v218, s[24:27], s74 offen lds\n"
      "v_mfma_f32_16x16x32_bf16 v[136:139], v[160:163], a[196:199], v[136:139]\n"
      "ds_read_b64_tr_b16 v[184:185], v232 offset:15360\n"
      "ds_read_b64_tr_b16 v[186:187], v232 offset:16128\n"
      "v_mfma_f32_16x16x32_bf16 v[132:135], v[164:167], a[192:195], v[132:135]\n"
      "v_add_f32 v193, v193, v197\n"
      "v_add_f32 v194, v194, v198\n"
      "v_mfma_f32_16x16x32_bf16 v[140:143], v[164:167], a[196:199], v[140:143]\n"
      "s_waitcnt lgkmcnt(9)\n"
      "ds_read_b64_tr_b16 v[188:189], v229 offset:15360\n"
      "ds_read_b64_tr_b16 v[190:191], v229 offset:16128\n"
      "v_mfma_f32_16x16x32_bf16 v[144:147], v[168:171], a[192:195], v[144:147]\n"
      "buffer_load_dwordx4 v219, s[24:27], s74 offen offset:1024 lds\n"
      "v_mfma_f32_16x16x32_bf16 v[152:155], v[168:171], a[196:199], v[152:155]\n"
      "s_add_u32 m0, s59, 0xc800\n"
      "ds_read_b64_tr_b16 v[160:161], v232 offset:12352\n"
      "ds_read_b64_tr_b16 v[162:163], v232 offset:13120\n"
      "v_mfma_f32_16x16x32_bf16 v[148:151], v[172:175], a[192:195], v[148:151]\n"
      "s_add_u32 s74, s81, 0\n"
      "v_add_f32 v195, v195, v199\n"
      "v_mul_f32 v44, 0xbb000000, v192\n"
      "v_mfma_f32_16x16x32_bf16 v[156:159], v[172:175], a[196:199], v[156:159]\n"
      "s_waitcnt vmcnt(16) lgkmcnt(6)\n"
      "ds_read_b64_tr_b16 v[164:165], v229 offset:12352\n"
      "ds_read_b64_tr_b16 v[166:167], v229 offset:13120\n"
      "v_mfma_f32_16x16x32_bf16 v[2:5], v[176:179], a[200:203], v[2:5]\n"
      "v_mul_f32 v193, 0x3b000000, v193\n"
      "v_fma_f32 v193, -v44, v44, v193\n"
      "v_mfma_f32_16x16x32_bf16 v[10:13], v[176:179], a[204:207], v[10:13]\n"
      "ds_read_b64_tr_b16 v[168:169], v232 offset:15424\n"
      "ds_read_b64_tr_b16 v[170:171], v232 offset:16192\n"
      "v_mfma_f32_16x16x32_bf16 v[6:9], v[180:183], a[200:203], v[6:9]\n"
      "v_dot2c_f32_bf16 v210, v48, v248\n"
      "v_dot2c_f32_bf16 v211, v52, v248\n"
      "v_dot2c_f32_bf16 v212, v56, v248\n"
      "v_dot2c_f32_bf16 v213, v60, v248\n"
      "v_dot2c_f32_bf16 v214, v64, v248\n"
      "v_dot2c_f32_bf16 v215, v68, v248\n"
      "v_dot2c_f32_bf16 v210, v49, v248\n"
      "v_dot2c_f32_bf16 v211, v53, v248\n"
      "v_dot2c_f32_bf16 v212, v57, v248\n"
      "v_dot2c_f32_bf16 v213, v61, v248\n"
      "v_dot2c_f32_bf16 v214, v65, v248\n"
      "v_dot2c_f32_bf16 v215, v69, v248\n"
      "v_dot2c_f32_bf16 v210, v50, v248\n"
      "v_dot2c_f32_bf16 v211, v54, v248\n"
      "v_dot2c_f32_bf16 v212, v58, v248\n"
      "v_dot2c_f32_bf16 v213, v62, v248\n"
      "v_dot2c_f32_bf16 v214, v66, v248\n"
      "v_dot2c_f32_bf16 v215, v70, v248\n"
      "v_dot2c_f32_bf16 v210, v51, v248\n"
      "v_dot2c_f32_bf16 v211, v55, v248\n"
      "v_dot2c_f32_bf16 v212, v59, v248\n"
      "v_dot2c_f32_bf16 v213, v63, v248\n"
      "v_dot2c_f32_bf16 v214, v67, v248\n"
      "v_dot2c_f32_bf16 v215, v71, v248\n"
      "v_dot2c_f32_bf16 v210, v72, v248\n"
      "v_dot2c_f32_bf16 v211, v76, v248\n"
      "v_dot2c_f32_bf16 v212, v80, v248\n"
      "v_dot2c_f32_bf16 v213, v84, v248\n"
      "v_dot2c_f32_bf16 v214, v88, v248\n"
      "v_dot2c_f32_bf16 v215, v92, v248\n"
      "v_dot2c_f32_bf16 v210, v73, v248\n"
      "v_dot2c_f32_bf16 v211, v77, v248\n"
      "v_dot2c_f32_bf16 v212, v81, v248\n"
      "v_dot2c_f32_bf16 v213, v85, v248\n"
      "v_dot2c_f32_bf16 v214, v89, v248\n"
      "v_dot2c_f32_bf16 v215, v93, v248\n"
      "v_dot2c_f32_bf16 v210, v74, v248\n"
      "v_dot2c_f32_bf16 v211, v78, v248\n"
      "v_dot2c_f32_bf16 v212, v82, v248\n"
      "v_dot2c_f32_bf16 v213, v86, v248\n"
      "v_dot2c_f32_bf16 v214, v90, v248\n"
      "v_dot2c_f32_bf16 v215, v94, v248\n"
      "v_dot2c_f32_bf16 v210, v75, v248\n"
      "v_dot2c_f32_bf16 v211, v79, v248\n"
      "v_dot2c_f32_bf16 v212, v83, v248\n"
      "v_dot2c_f32_bf16 v213, v87, v248\n"
      "v_dot2c_f32_bf16 v214, v91, v248\n"
      "v_dot2c_f32_bf16 v215, v95, v248\n"
      "v_permlane32_swap_b32 v210, v211\n"
      "v_add_f32 v210, v210, v211\n"
      "ds_write_b32 v209, v210\n"
      "v_permlane32_swap_b32 v212, v213\n"
      "v_add_f32 v212, v212, v213\n"
      "ds_write_b32 v209, v212 offset:256\n"
      "v_permlane32_swap_b32 v214, v215\n"
      "v_add_f32 v214, v214, v215\n"
      "ds_write_b32 v209, v214 offset:512\n"
      "v_mfma_f32_16x16x32_bf16 v[14:17], v[180:183], a[204:207], v[14:17]\n"
      "s_waitcnt lgkmcnt(9)\n"
      "ds_read_b64_tr_b16 v[172:173], v229 offset:15424\n"
      "ds_read_b64_tr_b16 v[174:175], v229 offset:16192\n"
      "v_mfma_f32_16x16x32_bf16 v[18:21], v[184:187], a[200:203], v[18:21]\n"
      "buffer_load_dwordx4 v218, s[24:27], s74 offen lds\n"
      "v_mfma_f32_16x16x32_bf16 v[26:29], v[184:187], a[204:207], v[26:29]\n"
      "ds_read_b64_tr_b16 v[176:177], v232 offset:12416\n"
      "ds_read_b64_tr_b16 v[178:179], v232 offset:13184\n"
      "v_mfma_f32_16x16x32_bf16 v[22:25], v[188:191], a[200:203], v[22:25]\n"
      "buffer_load_dwordx4 v219, s[24:27], s74 offen offset:1024 lds\n"
      "v_mfma_f32_16x16x32_bf16 v[30:33], v[188:191], a[204:207], v[30:33]\n"
      "s_waitcnt lgkmcnt(9)\n"
      "ds_read_b64_tr_b16 v[180:181], v229 offset:12416\n"
      "ds_read_b64_tr_b16 v[182:183], v229 offset:13184\n"
      "v_mfma_f32_16x16x32_bf16 v[96:99], v[160:163], a[200:203], v[96:99]\n"
      "s_waitcnt lgkmcnt(6)\n"
      "s_barrier\n"
      "s_add_u32 m0, s59, 0xd000\n"
      "v_mfma_f32_16x16x32_bf16 v[104:107], v[160:163], a[204:207], v[104:107]\n"
      "s_add_u32 s74, s81, 0x4000\n"
      "ds_read_b64_tr_b16 v[184:185], v232 offset:15488\n"
      "ds_read_b64_tr_b16 v[186:187], v232 offset:16256\n"
      "v_mfma_f32_16x16x32_bf16 v[100:103], v[164:167], a[200:203], v[100:103]\n"
      "ds_read_b32 v48, v208\n"
      "ds_read_b32 v49, v208 offset:768\n"
      "v_mfma_f32_16x16x32_bf16 v[108:111], v[164:167], a[204:207], v[108:111]\n"
      "s_waitcnt lgkmcnt(8)\n"
      "ds_read_b64_tr_b16 v[188:189], v229 offset:15488\n"
      "ds_read_b64_tr_b16 v[190:191], v229 offset:16256\n"
      "v_mfma_f32_16x16x32_bf16 v[112:115], v[168:171], a[200:203], v[112:115]\n"
      "buffer_load_dwordx4 v218, s[24:27], s74 offen lds\n"
      "v_mfma_f32_16x16x32_bf16 v[120:123], v[168:171], a[204:207], v[120:123]\n"
      "ds_read_b64_tr_b16 v[160:161], v232 offset:24576\n"
      "ds_read_b64_tr_b16 v[162:163], v232 offset:25344\n"
      "v_mfma_f32_16x16x32_bf16 v[116:119], v[172:175], a[200:203], v[116:119]\n"
      "ds_read_b32 v50, v208 offset:1536\n"
      "ds_read_b32 v51, v208 offset:2304\n"
      "v_mfma_f32_16x16x32_bf16 v[124:127], v[172:175], a[204:207], v[124:127]\n"
      "s_waitcnt lgkmcnt(10)\n"
      "ds_read_b64_tr_b16 v[164:165], v229 offset:24576\n"
      "ds_read_b64_tr_b16 v[166:167], v229 offset:25344\n"
      "v_mfma_f32_16x16x32_bf16 v[128:131], v[176:179], a[200:203], v[128:131]\n"
      "buffer_load_dwordx4 v219, s[24:27], s74 offen offset:1024 lds\n"
      "v_mfma_f32_16x16x32_bf16 v[136:139], v[176:179], a[204:207], v[136:139]\n"
      "s_add_u32 m0, s59, 0x12000\n"
      "ds_read_b64_tr_b16 v[168:169], v232 offset:27648\n"
      "ds_read_b64_tr_b16 v[170:171], v232 offset:28416\n"
      "v_mfma_f32_16x16x32_bf16 v[132:135], v[180:183], a[200:203], v[132:135]\n"
      "s_add_u32 s74, s82, 0\n"
      "ds_read_b32 v52, v208 offset:256\n"
      "s_waitcnt lgkmcnt(7)\n"
      "ds_read_b32 v53, v208 offset:1024\n"
      "v_mfma_f32_16x16x32_bf16 v[140:143], v[180:183], a[204:207], v[140:143]\n"
      "ds_read_b64_tr_b16 v[172:173], v229 offset:27648\n"
      "ds_read_b64_tr_b16 v[174:175], v229 offset:28416\n"
      "v_mfma_f32_16x16x32_bf16 v[144:147], v[184:187], a[200:203], v[144:147]\n"
      "ds_read_b32 v54, v208 offset:1792\n"
      "v_max_f32 v193, 0, v193\n"
      "v_mfma_f32_16x16x32_bf16 v[152:155], v[184:187], a[204:207], v[152:155]\n"
      "ds_read_b64_tr_b16 v[176:177], v232 offset:36864\n"
      "ds_read_b64_tr_b16 v[178:179], v232 offset:37632\n"
      "v_mfma_f32_16x16x32_bf16 v[148:151], v[188:191], a[200:203], v[148:151]\n"
      "buffer_load_dwordx4 v218, s[24:27], s74 offen lds\n"
      "v_mfma_f32_16x16x32_bf16 v[156:159], v[188:191], a[204:207], v[156:159]\n"
      "s_waitcnt vmcnt(19) lgkmcnt(9)\n"
      "ds_read_b64_tr_b16 v[180:181], v229 offset:36864\n"
      "ds_read_b64_tr_b16 v[182:183], v229 offset:37632\n"
      "v_mfma_f32_16x16x32_bf16 v[2:5], v[160:163], a[208:211], v[2:5]\n"
      "s_barrier\n"
      "ds_read_b32 v55, v208 offset:2560\n"
      "v_add_f32 v40, 0x3727c5ac, v193\n"
      "v_mfma_f32_16x16x32_bf16 v[10:13], v[160:163], a[212:215], v[10:13]\n"
      "ds_read_b64_tr_b16 v[184:185], v232 offset:39936\n"
      "ds_read_b64_tr_b16 v[186:187], v232 offset:40704\n"
      "v_mfma_f32_16x16x32_bf16 v[6:9], v[164:167], a[208:211], v[6:9]\n"
      "buffer_load_dwordx4 v219, s[24:27], s74 offen offset:1024 lds\n"
      "v_mfma_f32_16x16x32_bf16 v[14:17], v[164:167], a[212:215], v[14:17]\n"
      "s_waitcnt lgkmcnt(8)\n"
      "ds_read_b64_tr_b16 v[188:189], v229 offset:39936\n"
      "ds_read_b64_tr_b16 v[190:191], v229 offset:40704\n"
      "v_mfma_f32_16x16x32_bf16 v[18:21], v[168:171], a[208:211], v[18:21]\n"
      "s_add_u32 m0, s59, 0x12800\n"
      "s_add_u32 s74, s82, 0x4000\n"
      "ds_read_b32 v56, v208 offset:512\n"
      "v_mfma_f32_16x16x32_bf16 v[26:29], v[168:171], a[212:215], v[26:29]\n"
      "ds_read_b64_tr_b16 v[160:161], v230\n"
      "ds_read_b64_tr_b16 v[162:163], v230 offset:768\n"
      "v_mfma_f32_16x16x32_bf16 v[22:25], v[172:175], a[208:211], v[22:25]\n"
      "ds_read_b32 v57, v208 offset:1280\n"
      "ds_read_b32 v58, v208 offset:2048\n"
      "v_mfma_f32_16x16x32_bf16 v[30:33], v[172:175], a[212:215], v[30:33]\n"
      "s_waitcnt vmcnt(18) lgkmcnt(10)\n"
      "ds_read_b64_tr_b16 v[164:165], v227\n"
      "ds_read_b64_tr_b16 v[166:167], v227 offset:768\n"
      "ds_read_b32 v59, v208 offset:2816\n"
      "v_mfma_f32_16x16x32_bf16 v[2:5], v[176:179], a[216:219], v[2:5]\n"
      "buffer_load_dwordx4 v218, s[24:27], s74 offen lds\n"
      "v_mfma_f32_16x16x32_bf16 v[10:13], v[176:179], a[220:223], v[10:13]\n"
      "ds_read_b64_tr_b16 v[168:169], v230 offset:3072\n"
      "ds_read_b64_tr_b16 v[170:171], v230 offset:3840\n"
      "v_mfma_f32_16x16x32_bf16 v[6:9], v[180:183], a[216:219], v[6:9]\n"
      "v_rsq_f32 v42, v40\n"
      "v_mfma_f32_16x16x32_bf16 v[14:17], v[180:183], a[220:223], v[14:17]\n"
      "s_waitcnt lgkmcnt(10)\n"
      "ds_read_b64_tr_b16 v[172:173], v227 offset:3072\n"
      "ds_read_b64_tr_b16 v[174:175], v227 offset:3840\n"
      "v_mfma_f32_16x16x32_bf16 v[18:21], v[184:187], a[216:219], v[18:21]\n"
      "buffer_load_dwordx4 v219, s[24:27], s74 offen offset:1024 lds\n"
      "v_mfma_f32_16x16x32_bf16 v[26:29], v[184:187], a[220:223], v[26:29]\n"
      "s_add_u32 m0, s59, 0x13000\n"
      "ds_read_b64_tr_b16 v[176:177], v230 offset:12288\n"
      "ds_read_b64_tr_b16 v[178:179], v230 offset:13056\n"
      "v_mfma_f32_16x16x32_bf16 v[22:25], v[188:191], a[216:219], v[22:25]\n"
      "s_add_u32 s74, s83, 0\n"
      "v_mul_f32 v42, 0xbfb8aa3b, v42\n"
      "v_mul_f32 v45, 0xbb000000, v194\n"
      "v_mfma_f32_16x16x32_bf16 v[30:33], v[188:191], a[220:223], v[30:33]\n"
      "s_waitcnt vmcnt(18) lgkmcnt(7)\n"
      "ds_read_b64_tr_b16 v[180:181], v227 offset:12288\n"
      "ds_read_b64_tr_b16 v[182:183], v227 offset:13056\n"
      "v_mfma_f32_16x16x32_bf16 v[2:5], v[160:163], a[224:227], v[2:5]\n"
      "v_mul_f32 v195, 0x3b000000, v195\n"
      "v_fma_f32 v195, -v45, v45, v195\n"
      "v_mfma_f32_16x16x32_bf16 v[10:13], v[160:163], a[228:231], v[10:13]\n"
      "ds_read_b64_tr_b16 v[184:185], v230 offset:15360\n"
      "ds_read_b64_tr_b16 v[186:187], v230 offset:16128\n"
      "v_mfma_f32_16x16x32_bf16 v[6:9], v[164:167], a[224:227], v[6:9]\n"
      "buffer_load_dwordx4 v218, s[24:27], s74 offen lds\n"
      "v_mfma_f32_16x16x32_bf16 v[14:17], v[164:167], a[228:231], v[14:17]\n"
      "s_waitcnt lgkmcnt(6)\n"
      "ds_read_b64_tr_b16 v[188:189], v227 offset:15360\n"
      "ds_read_b64_tr_b16 v[190:191], v227 offset:16128\n"
      "v_mfma_f32_16x16x32_bf16 v[18:21], v[168:171], a[224:227], v[18:21]\n"
      "s_waitcnt vmcnt(11)\n"
      "ds_read_b64_tr_b16 a[0:1], v222\n"
      "v_max_f32 v195, 0, v195\n"
      "v_mfma_f32_16x16x32_bf16 v[26:29], v[168:171], a[228:231], v[26:29]\n"
      "ds_read_b64_tr_b16 v[160:161], v230 offset:24576\n"
      "ds_read_b64_tr_b16 v[162:163], v230 offset:25344\n"
      "v_mfma_f32_16x16x32_bf16 v[22:25], v[172:175], a[224:227], v[22:25]\n"
      "buffer_load_dwordx4 v219, s[24:27], s74 offen offset:1024 lds\n"
      "v_mfma_f32_16x16x32_bf16 v[30:33], v[172:175], a[228:231], v[30:33]\n"
      "s_waitcnt lgkmcnt(7)\n"
      "ds_read_b64_tr_b16 v[164:165], v227 offset:24576\n"
      "ds_read_b64_tr_b16 v[166:167], v227 offset:25344\n"
      "v_mfma_f32_16x16x32_bf16 v[2:5], v[176:179], a[232:235], v[2:5]\n"
      "s_add_u32 m0, s59, 0x18000\n"
      "s_add_u32 s74, s83, 0x4000\n"
      "ds_read_b64_tr_b16 a[4:5], v222 offset:8\n"
      "v_mfma_f32_16x16x32_bf16 v[10:13], v[176:179], a[236:239], v[10:13]\n"
      "ds_read_b64_tr_b16 v[168:169], v230 offset:27648\n"
      "ds_read_b64_tr_b16 v[170:171], v230 offset:28416\n"
      "v_mfma_f32_16x16x32_bf16 v[6:9], v[180:183], a[232:235], v[6:9]\n"
      "ds_read_b64_tr_b16 a[8:9], v222 offset:256\n"
      "ds_read_b64_tr_b16 a[12:13], v222 offset:264\n"
      "v_mfma_f32_16x16x32_bf16 v[14:17], v[180:183], a[236:239], v[14:17]\n"
      "s_waitcnt lgkmcnt(10)\n"
      "ds_read_b64_tr_b16 v[172:173], v227 offset:27648\n"
      "ds_read_b64_tr_b16 v[174:175], v227 offset:28416\n"
      "v_mfma_f32_16x16x32_bf16 v[18:21], v[184:187], a[232:235], v[18:21]\n"
      "buffer_load_dwordx4 v218, s[24:27], s74 offen lds\n"
      "v_mfma_f32_16x16x32_bf16 v[26:29], v[184:187], a[236:239], v[26:29]\n"
      "ds_read_b64_tr_b16 v[176:177], v230 offset:36864\n"
      "ds_read_b64_tr_b16 v[178:179], v230 offset:37632\n"
      "v_mfma_f32_16x16x32_bf16 v[22:25], v[188:191], a[232:235], v[22:25]\n"
      "s_waitcnt vmcnt(11)\n"
      "ds_read_b64_tr_b16 a[2:3], v223\n"
      "s_waitcnt lgkmcnt(7)\n"
      "ds_read_b64_tr_b16 a[6:7], v223 offset:8\n"
      "v_mfma_f32_16x16x32_bf16 v[30:33], v[188:191], a[236:239], v[30:33]\n"
      "ds_read_b64_tr_b16 v[180:181], v227 offset:36864\n"
      "ds_read_b64_tr_b16 v[182:183], v227 offset:37632\n"
      "v_add_f32 v48, v48, v49\n"
      "v_mfma_f32_16x16x32_bf16 v[2:5], v[160:163], a[240:243], v[2:5]\n"
      "buffer_load_dwordx4 v219, s[24:27], s74 offen offset:1024 lds\n"
      "v_add_f32 v50, v50, v51\n"
      "v_mfma_f32_16x16x32_bf16 v[10:13], v[160:163], a[244:247], v[10:13]\n"
      "s_add_u32 m0, s59, 0x18800\n"
      "ds_read_b64_tr_b16 v[184:185], v230 offset:39936\n"
      "ds_read_b64_tr_b16 v[186:187], v230 offset:40704\n"
      "v_add_f32 v48, v48, v50\n"
      "ds_write_b32 v209, v48 offset:3072\n"
      "v_mfma_f32_16x16x32_bf16 v[6:9], v[164:167], a[240:243], v[6:9]\n"
      "s_add_u32 s74, s80, 0x8000\n"
      "ds_read_b64_tr_b16 a[10:11], v223 offset:256\n"
      "ds_read_b64_tr_b16 a[14:15], v223 offset:264\n"
      "v_add_f32 v52, v52, v53\n"
      "v_mfma_f32_16x16x32_bf16 v[14:17], v[164:167], a[244:247], v[14:17]\n"
      "s_waitcnt lgkmcnt(11)\n"
      "ds_read_b64_tr_b16 v[188:189], v227 offset:39936\n"
      "ds_read_b64_tr_b16 v[190:191], v227 offset:40704\n"
      "v_add_f32 v54, v54, v55\n"
      "v_add_f32 v52, v52, v54\n"
      "v_mfma_f32_16x16x32_bf16 v[18:21], v[168:171], a[240:243], v[18:21]\n"
      "v_add_f32 v41, 0x3727c5ac, v195\n"
      "v_rsq_f32 v43, v41\n"
      "ds_write_b32 v209, v52 offset:3328\n"
      "v_mfma_f32_16x16x32_bf16 v[26:29], v[168:171], a[244:247], v[26:29]\n"
      "ds_read_b64_tr_b16 v[160:161], v232 offset:24640\n"
      "s_waitcnt lgkmcnt(7)\n"
      "ds_read_b64_tr_b16 v[162:163], v232 offset:25408\n"
      "v_add_f32 v56, v56, v57\n"
      "v_add_f32 v58, v58, v59\n"
      "v_mfma_f32_16x16x32_bf16 v[22:25], v[172:175], a[240:243], v[22:25]\n"
      "buffer_load_dwordx4 v218, s[24:27], s74 offen lds\n"
      "v_add_f32 v56, v56, v58\n"
      "v_mfma_f32_16x16x32_bf16 v[30:33], v[172:175], a[244:247], v[30:33]\n"
      "ds_read_b64_tr_b16 v[164:165], v229 offset:24640\n"
      "ds_read_b64_tr_b16 v[166:167], v229 offset:25408\n"
      "v_mul_f32 v43, 0xbfb8aa3b, v43\n"
      "ds_write_b32 v209, v56 offset:3584\n"
      "ds_read_b128 v[64:67], v46 offset:3072\n"
      "v_mfma_f32_16x16x32_bf16 v[2:5], v[176:179], a[248:251], v[2:5]\n"
      "ds_read_b128 v[68:71], v46 offset:3136\n"
      "v_mfma_f32_16x16x32_bf16 v[10:13], v[176:179], a[252:255], v[10:13]\n"
      "ds_read_b64_tr_b16 v[168:169], v232 offset:27712\n"
      "ds_read_b64_tr_b16 v[170:171], v232 offset:28480\n"
      "s_waitcnt lgkmcnt(7)\n"
      "ds_read_b128 v[72:75], v46 offset:3200\n"
      "ds_read_b128 v[76:79], v46 offset:3264\n"
      "v_mfma_f32_16x16x32_bf16 v[6:9], v[180:183], a[248:251], v[6:9]\n"
      "buffer_load_dwordx4 v219, s[24:27], s74 offen offset:1024 lds\n"
      "v_mfma_f32_16x16x32_bf16 v[14:17], v[180:183], a[252:255], v[14:17]\n"
      "s_add_u32 m0, s59, 0x19000\n"
      "ds_read_b64_tr_b16 v[172:173], v229 offset:27712\n"
      "ds_read_b64_tr_b16 v[174:175], v229 offset:28480\n"
      "v_mfma_f32_16x16x32_bf16 v[18:21], v[184:187], a[248:251], v[18:21]\n"
      "s_add_u32 s74, s80, 0xc000\n"
      "s_waitcnt vmcnt(12)\n"
      "ds_read_b64_tr_b16 a[16:17], v223 offset:2048\n"
      "v_mfma_f32_16x16x32_bf16 v[26:29], v[184:187], a[252:255], v[26:29]\n"
      "ds_read_b64_tr_b16 v[176:177], v232 offset:36928\n"
      "ds_read_b64_tr_b16 v[178:179], v232 offset:37696\n"
      "v_mfma_f32_16x16x32_bf16 v[22:25], v[188:191], a[248:251], v[22:25]\n"
      "ds_read_b64_tr_b16 a[20:21], v223 offset:2056\n"
      "s_waitcnt lgkmcnt(7)\n"
      "ds_read_b64_tr_b16 a[24:25], v223 offset:2304\n"
      "v_mfma_f32_16x16x32_bf16 v[30:33], v[188:191], a[252:255], v[30:33]\n"
      "ds_read_b64_tr_b16 v[180:181], v229 offset:36928\n"
      "ds_read_b64_tr_b16 v[182:183], v229 offset:37696\n"
      "v_pk_fma_f32 v[2:3], v[44:45], v[64:65], v[2:3] op_sel:[0,0,0] op_sel_hi:[0,1,1]\n"
      "v_pk_fma_f32 v[4:5], v[44:45], v[66:67], v[4:5] op_sel:[0,0,0] op_sel_hi:[0,1,1]\n"
      "v_pk_fma_f32 v[6:7], v[44:45], v[68:69], v[6:7] op_sel:[0,0,0] op_sel_hi:[0,1,1]\n"
      "v_pk_fma_f32 v[8:9], v[44:45], v[70:71], v[8:9] op_sel:[0,0,0] op_sel_hi:[0,1,1]\n"
      "ds_read_b64_tr_b16 a[28:29], v223 offset:2312\n"
      "s_waitcnt vmcnt(10)\n"
      "buffer_load_dwordx4 v218, s[24:27], s74 offen lds\n"
      "v_pk_mul_f32 v[80:81], v[2:3], v[42:43] op_sel:[0,0] op_sel_hi:[1,0]\n"
      "v_pk_mul_f32 v[82:83], v[4:5], v[42:43] op_sel:[0,0] op_sel_hi:[1,0]\n"
      "v_pk_mul_f32 v[84:85], v[6:7], v[42:43] op_sel:[0,0] op_sel_hi:[1,0]\n"
      "v_pk_mul_f32 v[86:87], v[8:9], v[42:43] op_sel:[0,0] op_sel_hi:[1,0]\n"
      "v_exp_f32 v80, v80\n"
      "v_exp_f32 v81, v81\n"
      "v_exp_f32 v82, v82\n"
      "v_exp_f32 v83, v83\n"
      "v_exp_f32 v84, v84\n"
      "ds_read_b64_tr_b16 a[18:19], v223 offset:4096\n"
      "ds_read_b64_tr_b16 a[22:23], v223 offset:4104\n"
      "buffer_load_dwordx4 v219, s[24:27], s74 offen offset:1024 lds\n"
      "v_exp_f32 v85, v85\n"
      "v_exp_f32 v86, v86\n"
      "v_exp_f32 v87, v87\n"
      "v_pk_fma_f32 v[18:19], v[44:45], v[72:73], v[18:19] op_sel:[0,0,0] op_sel_hi:[0,1,1]\n"
      "v_pk_fma_f32 v[20:21], v[44:45], v[74:75], v[20:21] op_sel:[0,0,0] op_sel_hi:[0,1,1]\n"
      "s_waitcnt lgkmcnt(12)\n"
      "v_pk_fma_f32 v[22:23], v[44:45], v[76:77], v[22:23] op_sel:[0,0,0] op_sel_hi:[0,1,1]\n"
      "v_pk_fma_f32 v[24:25], v[44:45], v[78:79], v[24:25] op_sel:[0,0,0] op_sel_hi:[0,1,1]\n"
      "v_pk_fma_f32 v[80:81], v[80:81], v[40:41], v[40:41] op_sel:[0,0,0] op_sel_hi:[1,0,0]\n"
      "v_pk_fma_f32 v[82:83], v[82:83], v[40:41], v[40:41] op_sel:[0,0,0] op_sel_hi:[1,0,0]\n"
      "v_pk_fma_f32 v[84:85], v[84:85], v[40:41], v[40:41] op_sel:[0,0,0] op_sel_hi:[1,0,0]\n"
      "v_pk_fma_f32 v[86:87], v[86:87], v[40:41], v[40:41] op_sel:[0,0,0] op_sel_hi:[1,0,0]\n"
      "ds_read_b64_tr_b16 a[26:27], v223 offset:4352\n"
      "ds_read_b64_tr_b16 a[30:31], v223 offset:4360\n"
      "s_add_u32 m0, s69, 0x24000\n"
      "s_add_u32 s74, s81, 0x8000\n"
      "buffer_load_dwordx4 v218, s[24:27], s74 offen lds\n"
      "v_rcp_f32 v80, v80\n"
      "v_rcp_f32 v81, v81\n"
      "v_rcp_f32 v82, v82\n"
      "v_rcp_f32 v83, v83\n"
      "v_rcp_f32 v84, v84\n"
      "v_rcp_f32 v85, v85\n"
      "v_rcp_f32 v86, v86\n"
      "v_rcp_f32 v87, v87\n"
      "s_waitcnt vmcnt(11)\n"
      "ds_read_b64_tr_b16 a[32:33], v223 offset:24576\n"
      "buffer_load_dwordx4 v219, s[24:27], s74 offen offset:1024 lds\n"
      "v_pk_mul_f32 v[2:3], v[2:3], v[18:19]\n"
      "v_pk_mul_f32 v[4:5], v[4:5], v[20:21]\n"
      "v_pk_mul_f32 v[6:7], v[6:7], v[22:23]\n"
      "v_pk_mul_f32 v[8:9], v[8:9], v[24:25]\n"
      "v_pk_mul_f32 v[2:3], v[2:3], v[80:81]\n"
      "v_pk_mul_f32 v[4:5], v[4:5], v[82:83]\n"
      "v_pk_mul_f32 v[6:7], v[6:7], v[84:85]\n"
      "v_pk_mul_f32 v[8:9], v[8:9], v[86:87]\n"
      "v_cvt_pk_bf16_f32 v88, v2, v3\n"
      "v_cvt_pk_bf16_f32 v89, v4, v5\n"
      "v_cvt_pk_bf16_f32 v90, v6, v7\n"
      "v_cvt_pk_bf16_f32 v91, v8, v9\n"
      "v_min_u32 v88, -2, v88\n"
      "v_min_u32 v89, -2, v89\n"
      "v_min_u32 v90, -2, v90\n"
      "v_min_u32 v91, -2, v91\n"
      "buffer_store_dwordx4 v[88:91], v255, s[48:51], s77 offen sc1\n"
      "v_pk_fma_f32 v[10:11], v[44:45], v[64:65], v[10:11] op_sel:[1,0,0] op_sel_hi:[1,1,1]\n"
      "v_pk_fma_f32 v[12:13], v[44:45], v[66:67], v[12:13] op_sel:[1,0,0] op_sel_hi:[1,1,1]\n"
      "v_pk_fma_f32 v[14:15], v[44:45], v[68:69], v[14:15] op_sel:[1,0,0] op_sel_hi:[1,1,1]\n"
      "v_pk_fma_f32 v[16:17], v[44:45], v[70:71], v[16:17] op_sel:[1,0,0] op_sel_hi:[1,1,1]\n"
      "s_waitcnt lgkmcnt(7)\n"
      "ds_read_b64_tr_b16 a[36:37], v223 offset:24584\n"
      "ds_read_b64_tr_b16 a[40:41], v223 offset:24832\n"
      "s_add_u32 m0, s59, 0xc000\n"
      "s_add_u32 s74, s81, 0xc000\n"
      "buffer_load_dwordx4 v218, s[24:27], s74 offen lds\n"
      "v_pk_mul_f32 v[80:81], v[10:11], v[42:43] op_sel:[0,1] op_sel_hi:[1,1]\n"
      "v_pk_mul_f32 v[82:83], v[12:13], v[42:43] op_sel:[0,1] op_sel_hi:[1,1]\n"
      "v_pk_mul_f32 v[84:85], v[14:15], v[42:43] op_sel:[0,1] op_sel_hi:[1,1]\n"
      "v_pk_mul_f32 v[86:87], v[16:17], v[42:43] op_sel:[0,1] op_sel_hi:[1,1]\n"
      "v_exp_f32 v80, v80\n"
      "v_exp_f32 v81, v81\n"
      "v_exp_f32 v82, v82\n"
      "v_exp_f32 v83, v83\n"
      "v_exp_f32 v84, v84\n"
      "ds_read_b64_tr_b16 a[44:45], v223 offset:24840\n"
      "s_waitcnt vmcnt(12)\n"
      "buffer_load_dwordx4 v219, s[24:27], s74 offen offset:1024 lds\n"
      "v_exp_f32 v85, v85\n"
      "v_exp_f32 v86, v86\n"
      "v_exp_f32 v87, v87\n"
      "v_pk_fma_f32 v[26:27], v[44:45], v[72:73], v[26:27] op_sel:[1,0,0] op_sel_hi:[1,1,1]\n"
      "v_pk_fma_f32 v[28:29], v[44:45], v[74:75], v[28:29] op_sel:[1,0,0] op_sel_hi:[1,1,1]\n"
      "v_pk_fma_f32 v[30:31], v[44:45], v[76:77], v[30:31] op_sel:[1,0,0] op_sel_hi:[1,1,1]\n"
      "v_pk_fma_f32 v[32:33], v[44:45], v[78:79], v[32:33] op_sel:[1,0,0] op_sel_hi:[1,1,1]\n"
      "v_pk_fma_f32 v[80:81], v[80:81], v[40:41], v[40:41] op_sel:[0,1,1] op_sel_hi:[1,1,1]\n"
      "v_pk_fma_f32 v[82:83], v[82:83], v[40:41], v[40:41] op_sel:[0,1,1] op_sel_hi:[1,1,1]\n"
      "v_pk_fma_f32 v[84:85], v[84:85], v[40:41], v[40:41] op_sel:[0,1,1] op_sel_hi:[1,1,1]\n"
      "v_pk_fma_f32 v[86:87], v[86:87], v[40:41], v[40:41] op_sel:[0,1,1] op_sel_hi:[1,1,1]\n"
      "ds_read_b64_tr_b16 a[34:35], v223 offset:26624\n"
      "ds_read_b64_tr_b16 a[38:39], v223 offset:26632\n"
      "s_add_u32 m0, s59, 0xc800\n"
      "s_add_u32 s74, s82, 0x8000\n"
      "s_waitcnt lgkmcnt(10)\n"
      "buffer_load_dwordx4 v218, s[24:27], s74 offen lds\n"
      "v_rcp_f32 v80, v80\n"
      "v_rcp_f32 v81, v81\n"
      "v_rcp_f32 v82, v82\n"
      "v_rcp_f32 v83, v83\n"
      "v_rcp_f32 v84, v84\n"
      "v_rcp_f32 v85, v85\n"
      "v_rcp_f32 v86, v86\n"
      "v_rcp_f32 v87, v87\n"
      "ds_read_b64_tr_b16 a[42:43], v223 offset:26880\n"
      "ds_read_b64_tr_b16 a[46:47], v223 offset:26888\n"
      "buffer_load_dwordx4 v219, s[24:27], s74 offen offset:1024 lds\n"
      "v_pk_mul_f32 v[10:11], v[10:11], v[26:27]\n"
      "v_pk_mul_f32 v[12:13], v[12:13], v[28:29]\n"
      "v_pk_mul_f32 v[14:15], v[14:15], v[30:31]\n"
      "v_pk_mul_f32 v[16:17], v[16:17], v[32:33]\n"
      "v_pk_mul_f32 v[10:11], v[10:11], v[80:81]\n"
      "v_pk_mul_f32 v[12:13], v[12:13], v[82:83]\n"
      "v_pk_mul_f32 v[14:15], v[14:15], v[84:85]\n"
      "v_pk_mul_f32 v[16:17], v[16:17], v[86:87]\n"
      "v_cvt_pk_bf16_f32 v92, v10, v11\n"
      "v_cvt_pk_bf16_f32 v93, v12, v13\n"
      "v_cvt_pk_bf16_f32 v94, v14, v15\n"
      "v_cvt_pk_bf16_f32 v95, v16, v17\n"
      "v_min_u32 v92, -2, v92\n"
      "v_min_u32 v93, -2, v93\n"
      "v_min_u32 v94, -2, v94\n"
      "v_min_u32 v95, -2, v95\n"
      "buffer_store_dwordx4 v[92:95], v233, s[48:51], s77 offen sc1\n"
      "v_mfma_f32_16x16x32_bf16 v[96:99], v[160:163], a[208:211], v[96:99]\n"
      "s_add_u32 m0, s59, 0xd000\n"
      "s_add_u32 s74, s82, 0xc000\n"
      "s_waitcnt vmcnt(14)\n"
      "v_mfma_f32_16x16x32_bf16 v[104:107], v[160:163], a[212:215], v[104:107]\n"
      "ds_read_b64_tr_b16 v[184:185], v232 offset:40000\n"
      "ds_read_b64_tr_b16 v[186:187], v232 offset:40768\n"
      "v_mfma_f32_16x16x32_bf16 v[100:103], v[164:167], a[208:211], v[100:103]\n"
      "s_waitcnt lgkmcnt(10)\n"
      "buffer_load_dwordx4 v218, s[24:27], s74 offen lds\n"
      "v_mfma_f32_16x16x32_bf16 v[108:111], v[164:167], a[212:215], v[108:111]\n"
      "ds_read_b64_tr_b16 v[188:189], v229 offset:40000\n"
      "ds_read_b64_tr_b16 v[190:191], v229 offset:40768\n"
      "v_mfma_f32_16x16x32_bf16 v[112:115], v[168:171], a[208:211], v[112:115]\n"
      "buffer_load_dwordx4 v219, s[24:27], s74 offen offset:1024 lds\n"
      "v_mfma_f32_16x16x32_bf16 v[120:123], v[168:171], a[212:215], v[120:123]\n"
      "s_add_u32 m0, s59, 0x12000\n"
      "ds_read_b64_tr_b16 v[160:161], v230 offset:64\n"
      "ds_read_b64_tr_b16 v[162:163], v230 offset:832\n"
      "v_mfma_f32_16x16x32_bf16 v[116:119], v[172:175], a[208:211], v[116:119]\n"
      "s_add_u32 s74, s83, 0x8000\n"
      "ds_read_b64_tr_b16 a[48:49], v223 offset:28672\n"
      "s_waitcnt lgkmcnt(7)\n"
      "ds_read_b64_tr_b16 a[52:53], v223 offset:28680\n"
      "v_mfma_f32_16x16x32_bf16 v[124:127], v[172:175], a[212:215], v[124:127]\n"
      "ds_read_b64_tr_b16 v[164:165], v227 offset:64\n"
      "ds_read_b64_tr_b16 v[166:167], v227 offset:832\n"
      "v_mfma_f32_16x16x32_bf16 v[96:99], v[176:179], a[216:219], v[96:99]\n"
      "ds_read_b64_tr_b16 a[56:57], v223 offset:28928\n"
      "v_mfma_f32_16x16x32_bf16 v[104:107], v[176:179], a[220:223], v[104:107]\n"
      "ds_read_b64_tr_b16 v[168:169], v230 offset:3136\n"
      "ds_read_b64_tr_b16 v[170:171], v230 offset:3904\n"
      "v_mfma_f32_16x16x32_bf16 v[100:103], v[180:183], a[216:219], v[100:103]\n"
      "buffer_load_dwordx4 v218, s[24:27], s74 offen lds\n"
      "v_mfma_f32_16x16x32_bf16 v[108:111], v[180:183], a[220:223], v[108:111]\n"
      "s_waitcnt lgkmcnt(9)\n"
      "ds_read_b64_tr_b16 v[172:173], v227 offset:3136\n"
      "ds_read_b64_tr_b16 v[174:175], v227 offset:3904\n"
      "v_mfma_f32_16x16x32_bf16 v[112:115], v[184:187], a[216:219], v[112:115]\n"
      "ds_read_b64_tr_b16 a[60:61], v223 offset:28936\n"
      "v_mfma_f32_16x16x32_bf16 v[120:123], v[184:187], a[220:223], v[120:123]\n"
      "ds_read_b64_tr_b16 v[176:177], v230 offset:12352\n"
      "ds_read_b64_tr_b16 v[178:179], v230 offset:13120\n"
      "v_mfma_f32_16x16x32_bf16 v[116:119], v[188:191], a[216:219], v[116:119]\n"
      "buffer_load_dwordx4 v219, s[24:27], s74 offen offset:1024 lds\n"
      "v_mfma_f32_16x16x32_bf16 v[124:127], v[188:191], a[220:223], v[124:127]\n"
      "s_waitcnt lgkmcnt(8)\n"
      "ds_read_b64_tr_b16 v[180:181], v227 offset:12352\n"
      "ds_read_b64_tr_b16 v[182:183], v227 offset:13120\n"
      "v_mfma_f32_16x16x32_bf16 v[96:99], v[160:163], a[224:227], v[96:99]\n"
      "s_add_u32 m0, s59, 0x12800\n"
      "s_add_u32 s74, s83, 0xc000\n"
      "ds_read_b128 v[64:67], v46 offset:3328\n"
      "v_mfma_f32_16x16x32_bf16 v[104:107], v[160:163], a[228:231], v[104:107]\n"
      "ds_read_b64_tr_b16 v[184:185], v230 offset:15424\n"
      "ds_read_b64_tr_b16 v[186:187], v230 offset:16192\n"
      "v_mfma_f32_16x16x32_bf16 v[100:103], v[164:167], a[224:227], v[100:103]\n"
      "ds_read_b128 v[68:71], v46 offset:3392\n"
      "v_mfma_f32_16x16x32_bf16 v[108:111], v[164:167], a[228:231], v[108:111]\n"
      "s_waitcnt lgkmcnt(9)\n"
      "ds_read_b64_tr_b16 v[188:189], v227 offset:15424\n"
      "ds_read_b64_tr_b16 v[190:191], v227 offset:16192\n"
      "v_mfma_f32_16x16x32_bf16 v[112:115], v[168:171], a[224:227], v[112:115]\n"
      "buffer_load_dwordx4 v218, s[24:27], s74 offen lds\n"
      "v_mfma_f32_16x16x32_bf16 v[120:123], v[168:171], a[228:231], v[120:123]\n"
      "ds_read_b64_tr_b16 v[160:161], v230 offset:24640\n"
      "ds_read_b64_tr_b16 v[162:163], v230 offset:25408\n"
      "v_mfma_f32_16x16x32_bf16 v[116:119], v[172:175], a[224:227], v[116:119]\n"
      "ds_read_b128 v[72:75], v46 offset:3456\n"
      "v_mfma_f32_16x16x32_bf16 v[124:127], v[172:175], a[228:231], v[124:127]\n"
      "s_waitcnt lgkmcnt(9)\n"
      "ds_read_b64_tr_b16 v[164:165], v227 offset:24640\n"
      "ds_read_b64_tr_b16 v[166:167], v227 offset:25408\n"
      "v_mfma_f32_16x16x32_bf16 v[96:99], v[176:179], a[232:235], v[96:99]\n"
      "buffer_load_dwordx4 v219, s[24:27], s74 offen offset:1024 lds\n"
      "v_mfma_f32_16x16x32_bf16 v[104:107], v[176:179], a[236:239], v[104:107]\n"
      "s_add_u32 m0, s59, 0x13000\n"
      "ds_read_b64_tr_b16 v[168:169], v230 offset:27712\n"
      "ds_read_b64_tr_b16 v[170:171], v230 offset:28480\n"
      "v_mfma_f32_16x16x32_bf16 v[100:103], v[180:183], a[232:235], v[100:103]\n"
      "s_add_u32 s74, s84, 0x10000\n"
      "ds_read_b128 v[76:79], v46 offset:3520\n"
      "v_mfma_f32_16x16x32_bf16 v[108:111], v[180:183], a[236:239], v[108:111]\n"
      "s_waitcnt lgkmcnt(8)\n"
      "ds_read_b64_tr_b16 v[172:173], v227 offset:27712\n"
      "ds_read_b64_tr_b16 v[174:175], v227 offset:28480\n"
      "v_mfma_f32_16x16x32_bf16 v[112:115], v[184:187], a[232:235], v[112:115]\n"
      "s_waitcnt vmcnt(18)\n"
      "ds_read_b64_tr_b16 a[50:51], v223 offset:49152\n"
      "v_mfma_f32_16x16x32_bf16 v[120:123], v[184:187], a[236:239], v[120:123]\n"
      "ds_read_b64_tr_b16 v[176:177], v230 offset:36928\n"
      "ds_read_b64_tr_b16 v[178:179], v230 offset:37696\n"
      "v_mfma_f32_16x16x32_bf16 v[116:119], v[188:191], a[232:235], v[116:119]\n"
      "buffer_load_dwordx4 v218, s[24:27], s74 offen lds\n"
      "v_mfma_f32_16x16x32_bf16 v[124:127], v[188:191], a[236:239], v[124:127]\n"
      "s_waitcnt lgkmcnt(8)\n"
      "ds_read_b64_tr_b16 v[180:181], v227 offset:36928\n"
      "ds_read_b64_tr_b16 v[182:183], v227 offset:37696\n"
      "v_mfma_f32_16x16x32_bf16 v[96:99], v[160:163], a[240:243], v[96:99]\n"
      "ds_read_b64_tr_b16 a[54:55], v223 offset:49160\n"
      "v_mfma_f32_16x16x32_bf16 v[104:107], v[160:163], a[244:247], v[104:107]\n"
      "ds_read_b64_tr_b16 v[184:185], v230 offset:40000\n"
      "ds_read_b64_tr_b16 v[186:187], v230 offset:40768\n"
      "v_mfma_f32_16x16x32_bf16 v[100:103], v[164:167], a[240:243], v[100:103]\n"
      "buffer_load_dwordx4 v219, s[24:27], s74 offen offset:1024 lds\n"
      "v_mfma_f32_16x16x32_bf16 v[108:111], v[164:167], a[244:247], v[108:111]\n"
      "s_waitcnt lgkmcnt(8)\n"
      "ds_read_b64_tr_b16 v[188:189], v227 offset:40000\n"
      "ds_read_b64_tr_b16 v[190:191], v227 offset:40768\n"
      "v_mfma_f32_16x16x32_bf16 v[112:115], v[168:171], a[240:243], v[112:115]\n"
      "ds_read_b64_tr_b16 a[58:59], v223 offset:49408\n"
      "v_mfma_f32_16x16x32_bf16 v[120:123], v[168:171], a[244:247], v[120:123]\n"
      "ds_read_b64_tr_b16 v[160:161], v232 offset:24704\n"
      "ds_read_b64_tr_b16 v[162:163], v232 offset:25472\n"
      "v_mfma_f32_16x16x32_bf16 v[116:119], v[172:175], a[240:243], v[116:119]\n"
      "ds_read_b64_tr_b16 a[62:63], v223 offset:49416\n"
      "s_waitcnt vmcnt(18)\n"
      "v_mfma_f32_16x16x32_bf16 v[124:127], v[172:175], a[244:247], v[124:127]\n"
      "s_waitcnt lgkmcnt(9)\n"
      "ds_read_b64_tr_b16 v[164:165], v229 offset:24704\n"
      "ds_read_b64_tr_b16 v[166:167], v229 offset:25472\n"
      "v_mfma_f32_16x16x32_bf16 v[96:99], v[176:179], a[248:251], v[96:99]\n"
      "s_add_u32 m0, s59, 0x18000\n"
      "s_add_u32 s74, s84, 0x14000\n"
      "ds_read_b64_tr_b16 a[64:65], v223 offset:51200\n"
      "v_mfma_f32_16x16x32_bf16 v[104:107], v[176:179], a[252:255], v[104:107]\n"
      "ds_read_b64_tr_b16 v[168:169], v232 offset:27776\n"
      "ds_read_b64_tr_b16 v[170:171], v232 offset:28544\n"
      "v_mfma_f32_16x16x32_bf16 v[100:103], v[180:183], a[248:251], v[100:103]\n"
      "s_waitcnt lgkmcnt(5)\n"
      "buffer_load_dwordx4 v218, s[24:27], s74 offen lds\n"
      "v_mfma_f32_16x16x32_bf16 v[108:111], v[180:183], a[252:255], v[108:111]\n"
      "ds_read_b64_tr_b16 v[172:173], v229 offset:27776\n"
      "ds_read_b64_tr_b16 v[174:175], v229 offset:28544\n"
      "v_mfma_f32_16x16x32_bf16 v[112:115], v[184:187], a[248:251], v[112:115]\n"
      "buffer_load_dwordx4 v219, s[24:27], s74 offen offset:1024 lds\n"
      "v_mfma_f32_16x16x32_bf16 v[120:123], v[184:187], a[252:255], v[120:123]\n"
      "ds_read_b64_tr_b16 v[176:177], v232 offset:36992\n"
      "ds_read_b64_tr_b16 v[178:179], v232 offset:37760\n"
      "v_mfma_f32_16x16x32_bf16 v[116:119], v[188:191], a[248:251], v[116:119]\n"
      "ds_read_b64_tr_b16 a[68:69], v223 offset:51208\n"
      "ds_read_b64_tr_b16 a[72:73], v223 offset:51456\n"
      "v_mfma_f32_16x16x32_bf16 v[124:127], v[188:191], a[252:255], v[124:127]\n"
      "s_waitcnt lgkmcnt(9)\n"
      "ds_read_b64_tr_b16 v[180:181], v229 offset:36992\n"
      "ds_read_b64_tr_b16 v[182:183], v229 offset:37760\n"
      "v_pk_fma_f32 v[96:97], v[44:45], v[64:65], v[96:97] op_sel:[0,0,0] op_sel_hi:[0,1,1]\n"
      "v_pk_fma_f32 v[98:99], v[44:45], v[66:67], v[98:99] op_sel:[0,0,0] op_sel_hi:[0,1,1]\n"
      "v_pk_fma_f32 v[100:101], v[44:45], v[68:69], v[100:101] op_sel:[0,0,0] op_sel_hi:[0,1,1]\n"
      "v_pk_fma_f32 v[102:103], v[44:45], v[70:71], v[102:103] op_sel:[0,0,0] op_sel_hi:[0,1,1]\n"
      "ds_read_b64_tr_b16 a[76:77], v223 offset:51464\n"
      "s_waitcnt vmcnt(18)\n"
      "s_add_u32 m0, s59, 0x18800\n"
      "s_add_u32 s74, s84, 0x28000\n"
      "s_waitcnt lgkmcnt(0)\n"
      "buffer_load_dwordx4 v218, s[24:27], s74 offen lds\n"
      "v_pk_mul_f32 v[80:81], v[96:97], v[42:43] op_sel:[0,0] op_sel_hi:[1,0]\n"
      "v_pk_mul_f32 v[82:83], v[98:99], v[42:43] op_sel:[0,0] op_sel_hi:[1,0]\n"
      "v_pk_mul_f32 v[84:85], v[100:101], v[42:43] op_sel:[0,0] op_sel_hi:[1,0]\n"
      "v_pk_mul_f32 v[86:87], v[102:103], v[42:43] op_sel:[0,0] op_sel_hi:[1,0]\n"
      "v_exp_f32 v80, v80\n"
      "v_exp_f32 v81, v81\n"
      "v_exp_f32 v82, v82\n"
      "v_exp_f32 v83, v83\n"
      "v_exp_f32 v84, v84\n"
      "ds_read_b64_tr_b16 a[66:67], v223 offset:53248\n"
      "ds_read_b64_tr_b16 a[70:71], v223 offset:53256\n"
      "buffer_load_dwordx4 v219, s[24:27], s74 offen offset:1024 lds\n"
      "v_exp_f32 v85, v85\n"
      "v_exp_f32 v86, v86\n"
      "v_exp_f32 v87, v87\n"
      "v_pk_fma_f32 v[112:113], v[44:45], v[72:73], v[112:113] op_sel:[0,0,0] op_sel_hi:[0,1,1]\n"
      "v_pk_fma_f32 v[114:115], v[44:45], v[74:75], v[114:115] op_sel:[0,0,0] op_sel_hi:[0,1,1]\n"
      "v_pk_fma_f32 v[116:117], v[44:45], v[76:77], v[116:117] op_sel:[0,0,0] op_sel_hi:[0,1,1]\n"
      "v_pk_fma_f32 v[118:119], v[44:45], v[78:79], v[118:119] op_sel:[0,0,0] op_sel_hi:[0,1,1]\n"
      "v_pk_fma_f32 v[80:81], v[80:81], v[40:41], v[40:41] op_sel:[0,0,0] op_sel_hi:[1,0,0]\n"
      "v_pk_fma_f32 v[82:83], v[82:83], v[40:41], v[40:41] op_sel:[0,0,0] op_sel_hi:[1,0,0]\n"
      "v_pk_fma_f32 v[84:85], v[84:85], v[40:41], v[40:41] op_sel:[0,0,0] op_sel_hi:[1,0,0]\n"
      "v_pk_fma_f32 v[86:87], v[86:87], v[40:41], v[40:41] op_sel:[0,0,0] op_sel_hi:[1,0,0]\n"
      "ds_read_b64_tr_b16 a[74:75], v223 offset:53504\n"
      "ds_read_b64_tr_b16 a[78:79], v223 offset:53512\n"
      "s_add_u32 m0, s59, 0x19000\n"
      "s_add_u32 s74, s84, 0x2c000\n"
      "s_waitcnt lgkmcnt(0)\n"
      "buffer_load_dwordx4 v218, s[24:27], s74 offen lds\n"
      "v_rcp_f32 v80, v80\n"
      "v_rcp_f32 v81, v81\n"
      "v_rcp_f32 v82, v82\n"
      "v_rcp_f32 v83, v83\n"
      "v_rcp_f32 v84, v84\n"
      "v_rcp_f32 v85, v85\n"
      "v_rcp_f32 v86, v86\n"
      "v_rcp_f32 v87, v87\n"
      "s_waitcnt vmcnt(19)\n"
      "ds_read_b64_tr_b16 a[80:81], v222\n"
      "buffer_load_dwordx4 v219, s[24:27], s74 offen offset:1024 lds\n"
      "v_pk_mul_f32 v[96:97], v[96:97], v[112:113]\n"
      "v_pk_mul_f32 v[98:99], v[98:99], v[114:115]\n"
      "v_pk_mul_f32 v[100:101], v[100:101], v[116:117]\n"
      "v_pk_mul_f32 v[102:103], v[102:103], v[118:119]\n"
      "v_pk_mul_f32 v[96:97], v[96:97], v[80:81]\n"
      "v_pk_mul_f32 v[98:99], v[98:99], v[82:83]\n"
      "v_pk_mul_f32 v[100:101], v[100:101], v[84:85]\n"
      "v_pk_mul_f32 v[102:103], v[102:103], v[86:87]\n"
      "v_cvt_pk_bf16_f32 v88, v96, v97\n"
      "v_cvt_pk_bf16_f32 v89, v98, v99\n"
      "v_cvt_pk_bf16_f32 v90, v100, v101\n"
      "v_cvt_pk_bf16_f32 v91, v102, v103\n"
      "v_min_u32 v88, -2, v88\n"
      "v_min_u32 v89, -2, v89\n"
      "v_min_u32 v90, -2, v90\n"
      "v_min_u32 v91, -2, v91\n"
      "buffer_store_dwordx4 v[88:91], v255, s[48:51], s78 offen sc1\n"
      "v_pk_fma_f32 v[104:105], v[44:45], v[64:65], v[104:105] op_sel:[1,0,0] op_sel_hi:[1,1,1]\n"
      "v_pk_fma_f32 v[106:107], v[44:45], v[66:67], v[106:107] op_sel:[1,0,0] op_sel_hi:[1,1,1]\n"
      "v_pk_fma_f32 v[108:109], v[44:45], v[68:69], v[108:109] op_sel:[1,0,0] op_sel_hi:[1,1,1]\n"
      "v_pk_fma_f32 v[110:111], v[44:45], v[70:71], v[110:111] op_sel:[1,0,0] op_sel_hi:[1,1,1]\n"
      "ds_read_b64_tr_b16 a[84:85], v222 offset:8\n"
      "ds_read_b64_tr_b16 a[88:89], v222 offset:256\n"
      "v_pk_mul_f32 v[80:81], v[104:105], v[42:43] op_sel:[0,1] op_sel_hi:[1,1]\n"
      "v_pk_mul_f32 v[82:83], v[106:107], v[42:43] op_sel:[0,1] op_sel_hi:[1,1]\n"
      "v_pk_mul_f32 v[84:85], v[108:109], v[42:43] op_sel:[0,1] op_sel_hi:[1,1]\n"
      "v_pk_mul_f32 v[86:87], v[110:111], v[42:43] op_sel:[0,1] op_sel_hi:[1,1]\n"
      "v_exp_f32 v80, v80\n"
      "v_exp_f32 v81, v81\n"
      "v_exp_f32 v82, v82\n"
      "v_exp_f32 v83, v83\n"
      "v_exp_f32 v84, v84\n"
      "ds_read_b64_tr_b16 a[92:93], v222 offset:264\n"
      "s_waitcnt vmcnt(18)\n"
      "s_add_u32 m0, s69, 0x24000\n"
      "s_add_u32 s74, s84, 0x40000\n"
      "s_waitcnt lgkmcnt(0)\n"
      "buffer_load_dwordx4 v220, s[24:27], s74 offen lds\n"
      "v_exp_f32 v85, v85\n"
      "v_exp_f32 v86, v86\n"
      "v_exp_f32 v87, v87\n"
      "v_pk_fma_f32 v[120:121], v[44:45], v[72:73], v[120:121] op_sel:[1,0,0] op_sel_hi:[1,1,1]\n"
      "v_pk_fma_f32 v[122:123], v[44:45], v[74:75], v[122:123] op_sel:[1,0,0] op_sel_hi:[1,1,1]\n"
      "v_pk_fma_f32 v[124:125], v[44:45], v[76:77], v[124:125] op_sel:[1,0,0] op_sel_hi:[1,1,1]\n"
      "v_pk_fma_f32 v[126:127], v[44:45], v[78:79], v[126:127] op_sel:[1,0,0] op_sel_hi:[1,1,1]\n"
      "v_pk_fma_f32 v[80:81], v[80:81], v[40:41], v[40:41] op_sel:[0,1,1] op_sel_hi:[1,1,1]\n"
      "v_pk_fma_f32 v[82:83], v[82:83], v[40:41], v[40:41] op_sel:[0,1,1] op_sel_hi:[1,1,1]\n"
      "v_pk_fma_f32 v[84:85], v[84:85], v[40:41], v[40:41] op_sel:[0,1,1] op_sel_hi:[1,1,1]\n"
      "v_pk_fma_f32 v[86:87], v[86:87], v[40:41], v[40:41] op_sel:[0,1,1] op_sel_hi:[1,1,1]\n"
      "ds_read_b64_tr_b16 a[82:83], v223\n"
      "ds_read_b64_tr_b16 a[86:87], v223 offset:8\n"
      "buffer_load_dwordx4 v221, s[24:27], s74 offen offset:1024 lds\n"
      "v_rcp_f32 v80, v80\n"
      "v_rcp_f32 v81, v81\n"
      "v_rcp_f32 v82, v82\n"
      "v_rcp_f32 v83, v83\n"
      "v_rcp_f32 v84, v84\n"
      "v_rcp_f32 v85, v85\n"
      "v_rcp_f32 v86, v86\n"
      "v_rcp_f32 v87, v87\n"
      "ds_read_b64_tr_b16 a[90:91], v223 offset:256\n"
      "ds_read_b64_tr_b16 a[94:95], v223 offset:264\n"
      "s_add_u32 m0, s59, 0xc000\n"
      "s_add_u32 s74, s84, 0x44000\n"
      "s_waitcnt lgkmcnt(0)\n"
      "buffer_load_dwordx4 v220, s[24:27], s74 offen lds\n"
      "v_pk_mul_f32 v[104:105], v[104:105], v[120:121]\n"
      "v_pk_mul_f32 v[106:107], v[106:107], v[122:123]\n"
      "v_pk_mul_f32 v[108:109], v[108:109], v[124:125]\n"
      "v_pk_mul_f32 v[110:111], v[110:111], v[126:127]\n"
      "v_pk_mul_f32 v[104:105], v[104:105], v[80:81]\n"
      "v_pk_mul_f32 v[106:107], v[106:107], v[82:83]\n"
      "v_pk_mul_f32 v[108:109], v[108:109], v[84:85]\n"
      "v_pk_mul_f32 v[110:111], v[110:111], v[86:87]\n"
      "v_cvt_pk_bf16_f32 v92, v104, v105\n"
      "v_cvt_pk_bf16_f32 v93, v106, v107\n"
      "v_cvt_pk_bf16_f32 v94, v108, v109\n"
      "v_cvt_pk_bf16_f32 v95, v110, v111\n"
      "v_min_u32 v92, -2, v92\n"
      "v_min_u32 v93, -2, v93\n"
      "v_min_u32 v94, -2, v94\n"
      "v_min_u32 v95, -2, v95\n"
      "buffer_store_dwordx4 v[92:95], v233, s[48:51], s78 offen sc1\n"
      "v_mfma_f32_16x16x32_bf16 v[128:131], v[160:163], a[208:211], v[128:131]\n"
      "ds_read_b128 v[64:67], v46 offset:3584\n"
      "v_mfma_f32_16x16x32_bf16 v[136:139], v[160:163], a[212:215], v[136:139]\n"
      "ds_read_b64_tr_b16 v[184:185], v232 offset:40064\n"
      "ds_read_b64_tr_b16 v[186:187], v232 offset:40832\n"
      "v_mfma_f32_16x16x32_bf16 v[132:135], v[164:167], a[208:211], v[132:135]\n"
      "buffer_load_dwordx4 v221, s[24:27], s74 offen offset:1024 lds\n"
      "v_mfma_f32_16x16x32_bf16 v[140:143], v[164:167], a[212:215], v[140:143]\n"
      "ds_read_b64_tr_b16 v[188:189], v229 offset:40064\n"
      "ds_read_b64_tr_b16 v[190:191], v229 offset:40832\n"
      "v_mfma_f32_16x16x32_bf16 v[144:147], v[168:171], a[208:211], v[144:147]\n"
      "ds_read_b128 v[68:71], v46 offset:3648\n"
      "v_mfma_f32_16x16x32_bf16 v[152:155], v[168:171], a[212:215], v[152:155]\n"
      "ds_read_b64_tr_b16 v[160:161], v230 offset:128\n"
      "ds_read_b64_tr_b16 v[162:163], v230 offset:896\n"
      "v_mfma_f32_16x16x32_bf16 v[148:151], v[172:175], a[208:211], v[148:151]\n"
      "buffer_load_dwordx4 v[192:195], v255, s[48:51], s86 offen sc1\n"
      "v_mfma_f32_16x16x32_bf16 v[156:159], v[172:175], a[212:215], v[156:159]\n"
      "ds_read_b64_tr_b16 v[164:165], v227 offset:128\n"
      "ds_read_b64_tr_b16 v[166:167], v227 offset:896\n"
      "v_mfma_f32_16x16x32_bf16 v[128:131], v[176:179], a[216:219], v[128:131]\n"
      "ds_read_b128 v[72:75], v46 offset:3712\n"
      "v_mfma_f32_16x16x32_bf16 v[136:139], v[176:179], a[220:223], v[136:139]\n"
      "ds_read_b64_tr_b16 v[168:169], v230 offset:3200\n"
      "ds_read_b64_tr_b16 v[170:171], v230 offset:3968\n"
      "v_mfma_f32_16x16x32_bf16 v[132:135], v[180:183], a[216:219], v[132:135]\n"
      "ds_read_b128 v[76:79], v46 offset:3776\n"
      "v_mfma_f32_16x16x32_bf16 v[140:143], v[180:183], a[220:223], v[140:143]\n"
      "s_waitcnt lgkmcnt(9)\n"
      "ds_read_b64_tr_b16 v[172:173], v227 offset:3200\n"
      "ds_read_b64_tr_b16 v[174:175], v227 offset:3968\n"
      "v_mfma_f32_16x16x32_bf16 v[144:147], v[184:187], a[216:219], v[144:147]\n"
      "buffer_load_dwordx4 v[196:199], v255, s[48:51], s86 offen offset:1024 sc1\n"
      "v_mfma_f32_16x16x32_bf16 v[152:155], v[184:187], a[220:223], v[152:155]\n"
      "s_add_u32 s4, s88, 0\n"
      "ds_read_b64_tr_b16 v[176:177], v230 offset:12416\n"
      "ds_read_b64_tr_b16 v[178:179], v230 offset:13184\n"
      "v_mfma_f32_16x16x32_bf16 v[148:151], v[188:191], a[216:219], v[148:151]\n"
      "v_mfma_f32_16x16x32_bf16 v[156:159], v[188:191], a[220:223], v[156:159]\n"
      "s_waitcnt lgkmcnt(8)\n"
      "ds_read_b64_tr_b16 v[180:181], v227 offset:12416\n"
      "ds_read_b64_tr_b16 v[182:183], v227 offset:13184\n"
      "v_mfma_f32_16x16x32_bf16 v[128:131], v[160:163], a[224:227], v[128:131]\n"
      "buffer_load_dwordx4 v[18:21], v255, s[48:51], s4 offen sc1\n"
      "v_mfma_f32_16x16x32_bf16 v[136:139], v[160:163], a[228:231], v[136:139]\n"
      "ds_read_b64_tr_b16 v[184:185], v230 offset:15488\n"
      "ds_read_b64_tr_b16 v[186:187], v230 offset:16256\n"
      "v_mfma_f32_16x16x32_bf16 v[132:135], v[164:167], a[224:227], v[132:135]\n"
      "v_mfma_f32_16x16x32_bf16 v[140:143], v[164:167], a[228:231], v[140:143]\n"
      "s_waitcnt lgkmcnt(6)\n"
      "ds_read_b64_tr_b16 v[188:189], v227 offset:15488\n"
      "ds_read_b64_tr_b16 v[190:191], v227 offset:16256\n"
      "v_mfma_f32_16x16x32_bf16 v[144:147], v[168:171], a[224:227], v[144:147]\n"
      "buffer_load_dwordx4 v[22:25], v255, s[48:51], s4 offen offset:1024 sc1\n"
      "v_mfma_f32_16x16x32_bf16 v[152:155], v[168:171], a[228:231], v[152:155]\n"
      "ds_read_b64_tr_b16 v[160:161], v230 offset:24704\n"
      "ds_read_b64_tr_b16 v[162:163], v230 offset:25472\n"
      "v_mfma_f32_16x16x32_bf16 v[148:151], v[172:175], a[224:227], v[148:151]\n"
      "v_mfma_f32_16x16x32_bf16 v[156:159], v[172:175], a[228:231], v[156:159]\n"
      "s_waitcnt lgkmcnt(6)\n"
      "ds_read_b64_tr_b16 v[164:165], v227 offset:24704\n"
      "ds_read_b64_tr_b16 v[166:167], v227 offset:25472\n"
      "v_mfma_f32_16x16x32_bf16 v[128:131], v[176:179], a[232:235], v[128:131]\n"
      "buffer_load_dwordx4 v[26:29], v255, s[48:51], s4 offen offset:2048 sc1\n"
      "v_mfma_f32_16x16x32_bf16 v[136:139], v[176:179], a[236:239], v[136:139]\n"
      "ds_read_b64_tr_b16 v[168:169], v230 offset:27776\n"
      "ds_read_b64_tr_b16 v[170:171], v230 offset:28544\n"
      "v_mfma_f32_16x16x32_bf16 v[132:135], v[180:183], a[232:235], v[132:135]\n"
      "v_mfma_f32_16x16x32_bf16 v[140:143], v[180:183], a[236:239], v[140:143]\n"
      "s_waitcnt lgkmcnt(6)\n"
      "ds_read_b64_tr_b16 v[172:173], v227 offset:27776\n"
      "ds_read_b64_tr_b16 v[174:175], v227 offset:28544\n"
      "v_mfma_f32_16x16x32_bf16 v[144:147], v[184:187], a[232:235], v[144:147]\n"
      "v_mfma_f32_16x16x32_bf16 v[152:155], v[184:187], a[236:239], v[152:155]\n"
      "ds_read_b64_tr_b16 v[176:177], v230 offset:36992\n"
      "ds_read_b64_tr_b16 v[178:179], v230 offset:37760\n"
      "v_mfma_f32_16x16x32_bf16 v[148:151], v[188:191], a[232:235], v[148:151]\n"
      "buffer_load_dwordx4 v[30:33], v255, s[48:51], s4 offen offset:3072 sc1\n"
      "v_mfma_f32_16x16x32_bf16 v[156:159], v[188:191], a[236:239], v[156:159]\n"
      "s_waitcnt lgkmcnt(6)\n"
      "ds_read_b64_tr_b16 v[180:181], v227 offset:36992\n"
      "ds_read_b64_tr_b16 v[182:183], v227 offset:37760\n"
      "v_mfma_f32_16x16x32_bf16 v[128:131], v[160:163], a[240:243], v[128:131]\n"
      "s_add_u32 s4, s87, 0\n"
      "v_mfma_f32_16x16x32_bf16 v[136:139], v[160:163], a[244:247], v[136:139]\n"
      "ds_read_b64_tr_b16 v[184:185], v230 offset:40064\n"
      "ds_read_b64_tr_b16 v[186:187], v230 offset:40832\n"
      "v_mfma_f32_16x16x32_bf16 v[132:135], v[164:167], a[240:243], v[132:135]\n"
      "buffer_load_dwordx4 v[2:5], v255, s[48:51], s4 offen sc1\n"
      "v_mfma_f32_16x16x32_bf16 v[140:143], v[164:167], a[244:247], v[140:143]\n"
      "s_waitcnt lgkmcnt(6)\n"
      "ds_read_b64_tr_b16 v[188:189], v227 offset:40064\n"
      "ds_read_b64_tr_b16 v[190:191], v227 offset:40832\n"
      "v_mfma_f32_16x16x32_bf16 v[144:147], v[168:171], a[240:243], v[144:147]\n"
      "v_mfma_f32_16x16x32_bf16 v[152:155], v[168:171], a[244:247], v[152:155]\n"
      "v_mfma_f32_16x16x32_bf16 v[148:151], v[172:175], a[240:243], v[148:151]\n"
      "buffer_load_dwordx4 v[6:9], v255, s[48:51], s4 offen offset:1024 sc1\n"
      "v_mfma_f32_16x16x32_bf16 v[156:159], v[172:175], a[244:247], v[156:159]\n"
      "s_waitcnt lgkmcnt(4)\n"
      "v_mfma_f32_16x16x32_bf16 v[128:131], v[176:179], a[248:251], v[128:131]\n"
      "v_mfma_f32_16x16x32_bf16 v[136:139], v[176:179], a[252:255], v[136:139]\n"
      "v_mfma_f32_16x16x32_bf16 v[132:135], v[180:183], a[248:251], v[132:135]\n"
      "buffer_load_dwordx4 v[10:13], v255, s[48:51], s4 offen offset:2048 sc1\n"
      "v_mfma_f32_16x16x32_bf16 v[140:143], v[180:183], a[252:255], v[140:143]\n"
      "s_waitcnt lgkmcnt(0)\n"
      "v_mfma_f32_16x16x32_bf16 v[144:147], v[184:187], a[248:251], v[144:147]\n"
      "v_mfma_f32_16x16x32_bf16 v[152:155], v[184:187], a[252:255], v[152:155]\n"
      "v_mfma_f32_16x16x32_bf16 v[148:151], v[188:191], a[248:251], v[148:151]\n"
      "v_mfma_f32_16x16x32_bf16 v[156:159], v[188:191], a[252:255], v[156:159]\n"
      "buffer_load_dwordx4 v[14:17], v255, s[48:51], s4 offen offset:3072 sc1\n"
      "v_pk_fma_f32 v[128:129], v[44:45], v[64:65], v[128:129] op_sel:[0,0,0] op_sel_hi:[0,1,1]\n"
      "v_pk_fma_f32 v[130:131], v[44:45], v[66:67], v[130:131] op_sel:[0,0,0] op_sel_hi:[0,1,1]\n"
      "v_pk_fma_f32 v[132:133], v[44:45], v[68:69], v[132:133] op_sel:[0,0,0] op_sel_hi:[0,1,1]\n"
      "v_pk_fma_f32 v[134:135], v[44:45], v[70:71], v[134:135] op_sel:[0,0,0] op_sel_hi:[0,1,1]\n"
      "s_add_u32 s4, s87, 0x1000\n"
      "buffer_load_dwordx4 v[96:99], v255, s[48:51], s4 offen sc1\n"
      "v_pk_mul_f32 v[80:81], v[128:129], v[42:43] op_sel:[0,0] op_sel_hi:[1,0]\n"
      "v_pk_mul_f32 v[82:83], v[130:131], v[42:43] op_sel:[0,0] op_sel_hi:[1,0]\n"
      "v_pk_mul_f32 v[84:85], v[132:133], v[42:43] op_sel:[0,0] op_sel_hi:[1,0]\n"
      "v_pk_mul_f32 v[86:87], v[134:135], v[42:43] op_sel:[0,0] op_sel_hi:[1,0]\n"
      "v_exp_f32 v80, v80\n"
      "v_exp_f32 v81, v81\n"
      "v_exp_f32 v82, v82\n"
      "v_exp_f32 v83, v83\n"
      "v_exp_f32 v84, v84\n"
      "buffer_load_dwordx4 v[100:103], v255, s[48:51], s4 offen offset:1024 sc1\n"
      "v_exp_f32 v85, v85\n"
      "v_exp_f32 v86, v86\n"
      "v_exp_f32 v87, v87\n"
      "v_pk_fma_f32 v[144:145], v[44:45], v[72:73], v[144:145] op_sel:[0,0,0] op_sel_hi:[0,1,1]\n"
      "v_pk_fma_f32 v[146:147], v[44:45], v[74:75], v[146:147] op_sel:[0,0,0] op_sel_hi:[0,1,1]\n"
      "v_pk_fma_f32 v[148:149], v[44:45], v[76:77], v[148:149] op_sel:[0,0,0] op_sel_hi:[0,1,1]\n"
      "v_pk_fma_f32 v[150:151], v[44:45], v[78:79], v[150:151] op_sel:[0,0,0] op_sel_hi:[0,1,1]\n"
      "v_pk_fma_f32 v[80:81], v[80:81], v[40:41], v[40:41] op_sel:[0,0,0] op_sel_hi:[1,0,0]\n"
      "v_pk_fma_f32 v[82:83], v[82:83], v[40:41], v[40:41] op_sel:[0,0,0] op_sel_hi:[1,0,0]\n"
      "v_pk_fma_f32 v[84:85], v[84:85], v[40:41], v[40:41] op_sel:[0,0,0] op_sel_hi:[1,0,0]\n"
      "v_pk_fma_f32 v[86:87], v[86:87], v[40:41], v[40:41] op_sel:[0,0,0] op_sel_hi:[1,0,0]\n"
      "buffer_load_dwordx4 v[104:107], v255, s[48:51], s4 offen offset:2048 sc1\n"
      "v_rcp_f32 v80, v80\n"
      "v_rcp_f32 v81, v81\n"
      "v_rcp_f32 v82, v82\n"
      "v_rcp_f32 v83, v83\n"
      "v_rcp_f32 v84, v84\n"
      "v_rcp_f32 v85, v85\n"
      "v_rcp_f32 v86, v86\n"
      "v_rcp_f32 v87, v87\n"
      "buffer_load_dwordx4 v[108:111], v255, s[48:51], s4 offen offset:3072 sc1\n"
      "v_pk_mul_f32 v[128:129], v[128:129], v[144:145]\n"
      "v_pk_mul_f32 v[130:131], v[130:131], v[146:147]\n"
      "v_pk_mul_f32 v[132:133], v[132:133], v[148:149]\n"
      "v_pk_mul_f32 v[134:135], v[134:135], v[150:151]\n"
      "v_pk_mul_f32 v[128:129], v[128:129], v[80:81]\n"
      "v_pk_mul_f32 v[130:131], v[130:131], v[82:83]\n"
      "v_pk_mul_f32 v[132:133], v[132:133], v[84:85]\n"
      "v_pk_mul_f32 v[134:135], v[134:135], v[86:87]\n"
      "v_cvt_pk_bf16_f32 v88, v128, v129\n"
      "v_cvt_pk_bf16_f32 v89, v130, v131\n"
      "v_cvt_pk_bf16_f32 v90, v132, v133\n"
      "v_cvt_pk_bf16_f32 v91, v134, v135\n"
      "v_min_u32 v88, -2, v88\n"
      "v_min_u32 v89, -2, v89\n"
      "v_min_u32 v90, -2, v90\n"
      "v_min_u32 v91, -2, v91\n"
      "buffer_store_dwordx4 v[88:91], v234, s[48:51], s79 offen sc1\n"
      "v_pk_fma_f32 v[136:137], v[44:45], v[64:65], v[136:137] op_sel:[1,0,0] op_sel_hi:[1,1,1]\n"
      "v_pk_fma_f32 v[138:139], v[44:45], v[66:67], v[138:139] op_sel:[1,0,0] op_sel_hi:[1,1,1]\n"
      "v_pk_fma_f32 v[140:141], v[44:45], v[68:69], v[140:141] op_sel:[1,0,0] op_sel_hi:[1,1,1]\n"
      "v_pk_fma_f32 v[142:143], v[44:45], v[70:71], v[142:143] op_sel:[1,0,0] op_sel_hi:[1,1,1]\n"
      "s_add_u32 s4, s88, 0x1000\n"
      "buffer_load_dwordx4 v[112:115], v255, s[48:51], s4 offen sc1\n"
      "v_pk_mul_f32 v[80:81], v[136:137], v[42:43] op_sel:[0,1] op_sel_hi:[1,1]\n"
      "v_pk_mul_f32 v[82:83], v[138:139], v[42:43] op_sel:[0,1] op_sel_hi:[1,1]\n"
      "v_pk_mul_f32 v[84:85], v[140:141], v[42:43] op_sel:[0,1] op_sel_hi:[1,1]\n"
      "v_pk_mul_f32 v[86:87], v[142:143], v[42:43] op_sel:[0,1] op_sel_hi:[1,1]\n"
      "v_exp_f32 v80, v80\n"
      "v_exp_f32 v81, v81\n"
      "v_exp_f32 v82, v82\n"
      "v_exp_f32 v83, v83\n"
      "v_exp_f32 v84, v84\n"
      "buffer_load_dwordx4 v[116:119], v255, s[48:51], s4 offen offset:1024 sc1\n"
      "v_exp_f32 v85, v85\n"
      "v_exp_f32 v86, v86\n"
      "v_exp_f32 v87, v87\n"
      "v_pk_fma_f32 v[152:153], v[44:45], v[72:73], v[152:153] op_sel:[1,0,0] op_sel_hi:[1,1,1]\n"
      "v_pk_fma_f32 v[154:155], v[44:45], v[74:75], v[154:155] op_sel:[1,0,0] op_sel_hi:[1,1,1]\n"
      "v_pk_fma_f32 v[156:157], v[44:45], v[76:77], v[156:157] op_sel:[1,0,0] op_sel_hi:[1,1,1]\n"
      "v_pk_fma_f32 v[158:159], v[44:45], v[78:79], v[158:159] op_sel:[1,0,0] op_sel_hi:[1,1,1]\n"
      "v_pk_fma_f32 v[80:81], v[80:81], v[40:41], v[40:41] op_sel:[0,1,1] op_sel_hi:[1,1,1]\n"
      "v_pk_fma_f32 v[82:83], v[82:83], v[40:41], v[40:41] op_sel:[0,1,1] op_sel_hi:[1,1,1]\n"
      "v_pk_fma_f32 v[84:85], v[84:85], v[40:41], v[40:41] op_sel:[0,1,1] op_sel_hi:[1,1,1]\n"
      "v_pk_fma_f32 v[86:87], v[86:87], v[40:41], v[40:41] op_sel:[0,1,1] op_sel_hi:[1,1,1]\n"
      "buffer_load_dwordx4 v[120:123], v255, s[48:51], s4 offen offset:2048 sc1\n"
      "v_rcp_f32 v80, v80\n"
      "v_rcp_f32 v81, v81\n"
      "v_rcp_f32 v82, v82\n"
      "v_rcp_f32 v83, v83\n"
      "v_rcp_f32 v84, v84\n"
      "v_rcp_f32 v85, v85\n"
      "v_rcp_f32 v86, v86\n"
      "v_rcp_f32 v87, v87\n"
      "buffer_load_dwordx4 v[124:127], v255, s[48:51], s4 offen offset:3072 sc1\n"
      "v_pk_mul_f32 v[136:137], v[136:137], v[152:153]\n"
      "v_pk_mul_f32 v[138:139], v[138:139], v[154:155]\n"
      "v_pk_mul_f32 v[140:141], v[140:141], v[156:157]\n"
      "v_pk_mul_f32 v[142:143], v[142:143], v[158:159]\n"
      "v_pk_mul_f32 v[136:137], v[136:137], v[80:81]\n"
      "v_pk_mul_f32 v[138:139], v[138:139], v[82:83]\n"
      "v_pk_mul_f32 v[140:141], v[140:141], v[84:85]\n"
      "v_pk_mul_f32 v[142:143], v[142:143], v[86:87]\n"
      "v_cvt_pk_bf16_f32 v92, v136, v137\n"
      "v_cvt_pk_bf16_f32 v93, v138, v139\n"
      "v_cvt_pk_bf16_f32 v94, v140, v141\n"
      "v_cvt_pk_bf16_f32 v95, v142, v143\n"
      "v_min_u32 v92, -2, v92\n"
      "v_min_u32 v93, -2, v93\n"
      "v_min_u32 v94, -2, v94\n"
      "v_min_u32 v95, -2, v95\n"
      "buffer_store_dwordx4 v[92:95], v235, s[48:51], s79 offen sc1\n"
      "s_waitcnt vmcnt(14)\n"
      "v_max3_u32 v239, v18, v19, v20\n"
      "v_max3_u32 v239, v239, v21, v22\n"
      "v_max3_u32 v239, v239, v23, v24\n"
      "v_max3_u32 v239, v239, v25, v26\n"
      "v_max3_u32 v239, v239, v27, v28\n"
      "v_max3_u32 v239, v239, v29, v30\n"
      "v_max3_u32 v239, v239, v31, v32\n"
      "v_max_u32 v239, v239, v33\n"
      "v_cmp_eq_u32 vcc, -1, v239\n"
      "s_nop 0\n"
      "s_cbranch_vccnz L13_%=\n"
      "L14_%=:\n"
      "v_mfma_f32_16x16x32_bf16 a[192:195], a[0:3], v[18:21], 0\n"
      "s_add_u32 s4, s87, 0x10000\n"
      "v_lshlrev_b32 v48, 16, v192\n"
      "v_and_b32 v49, 0xffff0000, v192\n"
      "v_mfma_f32_16x16x32_bf16 a[196:199], a[4:7], v[18:21], 0\n"
      "v_lshlrev_b32 v50, 16, v193\n"
      "v_and_b32 v51, 0xffff0000, v193\n"
      "v_mfma_f32_16x16x32_bf16 a[208:211], a[8:11], v[18:21], 0\n"
      "v_lshlrev_b32 v52, 16, v194\n"
      "v_and_b32 v53, 0xffff0000, v194\n"
      "v_mfma_f32_16x16x32_bf16 a[212:215], a[12:15], v[18:21], 0\n"
      "v_lshlrev_b32 v54, 16, v195\n"
      "v_and_b32 v55, 0xffff0000, v195\n"
      "v_mfma_f32_16x16x32_bf16 a[192:195], a[16:19], v[22:25], a[192:195]\n"
      "v_lshlrev_b32 v56, 16, v196\n"
      "v_and_b32 v57, 0xffff0000, v196\n"
      "v_mfma_f32_16x16x32_bf16 a[196:199], a[20:23], v[22:25], a[196:199]\n"
      "v_lshlrev_b32 v58, 16, v197\n"
      "v_and_b32 v59, 0xffff0000, v197\n"
      "v_mfma_f32_16x16x32_bf16 a[208:211], a[24:27], v[22:25], a[208:211]\n"
      "v_lshlrev_b32 v60, 16, v198\n"
      "v_and_b32 v61, 0xffff0000, v198\n"
      "v_mfma_f32_16x16x32_bf16 a[212:215], a[28:31], v[22:25], a[212:215]\n"
      "v_lshlrev_b32 v62, 16, v199\n"
      "v_and_b32 v63, 0xffff0000, v199\n"
      "s_waitcnt vmcnt(10)\n"
      "v_mfma_f32_16x16x32_bf16 a[192:195], a[32:35], v[26:29], a[192:195]\n"
      "v_max3_u32 v239, v2, v3, v4\n"
      "v_max3_u32 v239, v239, v5, v6\n"
      "v_mfma_f32_16x16x32_bf16 a[196:199], a[36:39], v[26:29], a[196:199]\n"
      "v_max3_u32 v239, v239, v7, v8\n"
      "v_max3_u32 v239, v239, v9, v10\n"
      "v_mfma_f32_16x16x32_bf16 a[208:211], a[40:43], v[26:29], a[208:211]\n"
      "v_max3_u32 v239, v239, v11, v12\n"
      "v_max3_u32 v239, v239, v13, v14\n"
      "v_mfma_f32_16x16x32_bf16 a[212:215], a[44:47], v[26:29], a[212:215]\n"
      "v_max3_u32 v239, v239, v15, v16\n"
      "v_max_u32 v239, v239, v17\n"
      "v_mfma_f32_16x16x32_bf16 a[192:195], a[48:51], v[30:33], a[192:195]\n"
      "buffer_load_dwordx4 v[160:163], v255, s[48:51], s4 offen sc1\n"
      "v_mfma_f32_16x16x32_bf16 a[196:199], a[52:55], v[30:33], a[196:199]\n"
      "buffer_load_dwordx4 v[164:167], v255, s[48:51], s4 offen offset:1024 sc1\n"
      "v_cmp_eq_u32 vcc, -1, v239\n"
      "s_nop 0\n"
      "s_cbranch_vccnz L15_%=\n"
      "L16_%=:\n"
      "v_mfma_f32_16x16x32_bf16 a[208:211], a[56:59], v[30:33], a[208:211]\n"
      "ds_read_b64_tr_b16 a[96:97], v223 offset:2048\n"
      "v_mfma_f32_16x16x32_bf16 a[212:215], a[60:63], v[30:33], a[212:215]\n"
      "ds_read_b64_tr_b16 a[100:101], v223 offset:2056\n"
      "v_mfma_f32_16x16x32_bf16 v[48:51], a[0:3], v[2:5], v[48:51]\n"
      "ds_read_b64_tr_b16 a[104:105], v223 offset:2304\n"
      "v_mfma_f32_16x16x32_bf16 v[52:55], a[4:7], v[2:5], v[52:55]\n"
      "buffer_load_dwordx4 v[168:171], v255, s[48:51], s4 offen offset:2048 sc1\n"
      "v_mfma_f32_16x16x32_bf16 a[176:179], a[8:11], v[2:5], 0\n"
      "ds_read_b64_tr_b16 a[108:109], v223 offset:2312\n"
      "v_mfma_f32_16x16x32_bf16 a[180:183], a[12:15], v[2:5], 0\n"
      "ds_read_b64_tr_b16 a[98:99], v223 offset:4096\n"
      "v_mfma_f32_16x16x32_bf16 v[48:51], a[16:19], v[6:9], v[48:51]\n"
      "buffer_load_dwordx4 v[172:175], v255, s[48:51], s4 offen offset:3072 sc1\n"
      "v_mfma_f32_16x16x32_bf16 v[52:55], a[20:23], v[6:9], v[52:55]\n"
      "s_add_u32 s4, s88, 0x10000\n"
      "ds_read_b64_tr_b16 a[102:103], v223 offset:4104\n"
      "v_mfma_f32_16x16x32_bf16 a[176:179], a[24:27], v[6:9], a[176:179]\n"
      "s_waitcnt vmcnt(10)\n"
      "v_max3_u32 v239, v96, v97, v98\n"
      "v_max3_u32 v239, v239, v99, v100\n"
      "v_mfma_f32_16x16x32_bf16 a[180:183], a[28:31], v[6:9], a[180:183]\n"
      "v_max3_u32 v239, v239, v101, v102\n"
      "v_max3_u32 v239, v239, v103, v104\n"
      "v_mfma_f32_16x16x32_bf16 v[48:51], a[32:35], v[10:13], v[48:51]\n"
      "buffer_load_dwordx4 v[176:179], v255, s[48:51], s4 offen sc1\n"
      "v_mfma_f32_16x16x32_bf16 v[52:55], a[36:39], v[10:13], v[52:55]\n"
      "v_max3_u32 v239, v239, v105, v106\n"
      "v_max3_u32 v239, v239, v107, v108\n"
      "v_mfma_f32_16x16x32_bf16 a[176:179], a[40:43], v[10:13], a[176:179]\n"
      "v_max3_u32 v239, v239, v109, v110\n"
      "v_max_u32 v239, v239, v111\n"
      "v_mfma_f32_16x16x32_bf16 a[180:183], a[44:47], v[10:13], a[180:183]\n"
      "v_cmp_eq_u32 vcc, -1, v239\n"
      "s_nop 0\n"
      "s_cbranch_vccnz L17_%=\n"
      "L18_%=:\n"
      "v_mfma_f32_16x16x32_bf16 v[48:51], a[48:51], v[14:17], v[48:51]\n"
      "buffer_load_dwordx4 v[180:183], v255, s[48:51], s4 offen offset:1024 sc1\n"
      "v_mfma_f32_16x16x32_bf16 v[52:55], a[52:55], v[14:17], v[52:55]\n"
      "ds_read_b64_tr_b16 a[106:107], v223 offset:4352\n"
      "v_mfma_f32_16x16x32_bf16 a[176:179], a[56:59], v[14:17], a[176:179]\n"
      "ds_read_b64_tr_b16 a[110:111], v223 offset:4360\n"
      "v_mfma_f32_16x16x32_bf16 a[180:183], a[60:63], v[14:17], a[180:183]\n"
      "buffer_load_dwordx4 v[184:187], v255, s[48:51], s4 offen offset:2048 sc1\n"
      "v_mfma_f32_16x16x32_bf16 v[56:59], a[0:3], v[96:99], v[56:59]\n"
      "ds_read_b64_tr_b16 a[112:113], v223 offset:24576\n"
      "v_mfma_f32_16x16x32_bf16 v[60:63], a[4:7], v[96:99], v[60:63]\n"
      "ds_read_b64_tr_b16 a[116:117], v223 offset:24584\n"
      "v_mfma_f32_16x16x32_bf16 a[184:187], a[8:11], v[96:99], 0\n"
      "ds_read_b64_tr_b16 a[120:121], v223 offset:24832\n"
      "v_mfma_f32_16x16x32_bf16 a[188:191], a[12:15], v[96:99], 0\n"
      "buffer_load_dwordx4 v[188:191], v255, s[48:51], s4 offen offset:3072 sc1\n"
      "v_mfma_f32_16x16x32_bf16 v[56:59], a[16:19], v[100:103], v[56:59]\n"
      "s_add_u32 s4, s88, 0x11000\n"
      "ds_read_b64_tr_b16 a[124:125], v223 offset:24840\n"
      "v_mfma_f32_16x16x32_bf16 v[60:63], a[20:23], v[100:103], v[60:63]\n"
      "ds_read_b64_tr_b16 a[114:115], v223 offset:26624\n"
      "v_mfma_f32_16x16x32_bf16 a[184:187], a[24:27], v[100:103], a[184:187]\n"
      "buffer_load_dwordx4 v[144:147], v255, s[48:51], s4 offen sc1\n"
      "v_mfma_f32_16x16x32_bf16 a[188:191], a[28:31], v[100:103], a[188:191]\n"
      "s_waitcnt vmcnt(10)\n"
      "v_max3_u32 v239, v112, v113, v114\n"
      "v_max3_u32 v239, v239, v115, v116\n"
      "v_mfma_f32_16x16x32_bf16 v[56:59], a[32:35], v[104:107], v[56:59]\n"
      "v_max3_u32 v239, v239, v117, v118\n"
      "v_max3_u32 v239, v239, v119, v120\n"
      "v_mfma_f32_16x16x32_bf16 v[60:63], a[36:39], v[104:107], v[60:63]\n"
      "v_max3_u32 v239, v239, v121, v122\n"
      "v_max3_u32 v239, v239, v123, v124\n"
      "v_mfma_f32_16x16x32_bf16 a[184:187], a[40:43], v[104:107], a[184:187]\n"
      "buffer_load_dwordx4 v[148:151], v255, s[48:51], s4 offen offset:1024 sc1\n"
      "v_mfma_f32_16x16x32_bf16 a[188:191], a[44:47], v[104:107], a[188:191]\n"
      "v_max3_u32 v239, v239, v125, v126\n"
      "v_max_u32 v239, v239, v127\n"
      "v_mfma_f32_16x16x32_bf16 v[56:59], a[48:51], v[108:111], v[56:59]\n"
      "v_cmp_eq_u32 vcc, -1, v239\n"
      "s_nop 0\n"
      "s_cbranch_vccnz L19_%=\n"
      "L20_%=:\n"
      "v_mfma_f32_16x16x32_bf16 v[60:63], a[52:55], v[108:111], v[60:63]\n"
      "ds_read_b64_tr_b16 a[118:119], v223 offset:26632\n"
      "v_mfma_f32_16x16x32_bf16 a[184:187], a[56:59], v[108:111], a[184:187]\n"
      "buffer_load_dwordx4 v[152:155], v255, s[48:51], s4 offen offset:2048 sc1\n"
      "v_mfma_f32_16x16x32_bf16 a[188:191], a[60:63], v[108:111], a[188:191]\n"
      "ds_read_b64_tr_b16 a[122:123], v223 offset:26880\n"
      "v_mfma_f32_16x16x32_bf16 a[200:203], a[0:3], v[112:115], 0\n"
      "s_waitcnt lgkmcnt(7)\n"
      "ds_read_b64_tr_b16 a[126:127], v223 offset:26888\n"
      "v_mfma_f32_16x16x32_bf16 a[204:207], a[4:7], v[112:115], 0\n"
      "buffer_load_dwordx4 v[156:159], v255, s[48:51], s4 offen offset:3072 sc1\n"
      "v_mfma_f32_16x16x32_bf16 a[216:219], a[8:11], v[112:115], 0\n"
      "s_add_u32 s4, s87, 0x11000\n"
      "ds_read_b64_tr_b16 a[128:129], v223 offset:28672\n"
      "v_mfma_f32_16x16x32_bf16 a[220:223], a[12:15], v[112:115], 0\n"
      "ds_read_b64_tr_b16 a[132:133], v223 offset:28680\n"
      "v_mfma_f32_16x16x32_bf16 a[200:203], a[16:19], v[116:119], a[200:203]\n"
      "ds_read_b64_tr_b16 a[136:137], v223 offset:28928\n"
      "v_mfma_f32_16x16x32_bf16 a[204:207], a[20:23], v[116:119], a[204:207]\n"
      "buffer_load_dwordx4 v[128:131], v255, s[48:51], s4 offen sc1\n"
      "v_mfma_f32_16x16x32_bf16 a[216:219], a[24:27], v[116:119], a[216:219]\n"
      "s_waitcnt vmcnt(9)\n"
      "v_max3_u32 v239, v160, v161, v162\n"
      "v_max3_u32 v239, v239, v163, v164\n"
      "v_mfma_f32_16x16x32_bf16 a[220:223], a[28:31], v[116:119], a[220:223]\n"
      "v_max3_u32 v239, v239, v165, v166\n"
      "v_max3_u32 v239, v239, v167, v168\n"
      "v_mfma_f32_16x16x32_bf16 a[200:203], a[32:35], v[120:123], a[200:203]\n"
      "buffer_load_dwordx4 v[132:135], v255, s[48:51], s4 offen offset:1024 sc1\n"
      "v_mfma_f32_16x16x32_bf16 a[204:207], a[36:39], v[120:123], a[204:207]\n"
      "v_max3_u32 v239, v239, v169, v170\n"
      "v_max3_u32 v239, v239, v171, v172\n"
      "v_mfma_f32_16x16x32_bf16 a[216:219], a[40:43], v[120:123], a[216:219]\n"
      "v_max3_u32 v239, v239, v173, v174\n"
      "v_max_u32 v239, v239, v175\n"
      "v_mfma_f32_16x16x32_bf16 a[220:223], a[44:47], v[120:123], a[220:223]\n"
      "v_cmp_eq_u32 vcc, -1, v239\n"
      "s_nop 0\n"
      "s_cbranch_vccnz L21_%=\n"
      "L22_%=:\n"
      "v_mfma_f32_16x16x32_bf16 a[200:203], a[48:51], v[124:127], a[200:203]\n"
      "buffer_load_dwordx4 v[136:139], v255, s[48:51], s4 offen offset:2048 sc1\n"
      "v_mfma_f32_16x16x32_bf16 a[204:207], a[52:55], v[124:127], a[204:207]\n"
      "ds_read_b64_tr_b16 a[140:141], v223 offset:28936\n"
      "v_mfma_f32_16x16x32_bf16 a[216:219], a[56:59], v[124:127], a[216:219]\n"
      "ds_read_b64_tr_b16 a[130:131], v223 offset:49152\n"
      "v_mfma_f32_16x16x32_bf16 a[220:223], a[60:63], v[124:127], a[220:223]\n"
      "ds_read_b64_tr_b16 a[134:135], v223 offset:49160\n"
      "v_mfma_f32_16x16x32_bf16 v[48:51], a[64:67], v[160:163], v[48:51]\n"
      "buffer_load_dwordx4 v[140:143], v255, s[48:51], s4 offen offset:3072 sc1\n"
      "v_mfma_f32_16x16x32_bf16 v[52:55], a[68:71], v[160:163], v[52:55]\n"
      "s_add_u32 s4, s88, 0x20000\n"
      "ds_read_b64_tr_b16 a[138:139], v223 offset:49408\n"
      "v_mfma_f32_16x16x32_bf16 a[176:179], a[72:75], v[160:163], a[176:179]\n"
      "s_waitcnt lgkmcnt(7)\n"
      "ds_read_b64_tr_b16 a[142:143], v223 offset:49416\n"
      "v_mfma_f32_16x16x32_bf16 a[180:183], a[76:79], v[160:163], a[180:183]\n"
      "ds_read_b64_tr_b16 a[144:145], v223 offset:51200\n"
      "v_mfma_f32_16x16x32_bf16 v[48:51], a[80:83], v[164:167], v[48:51]\n"
      "ds_read_b64_tr_b16 a[148:149], v223 offset:51208\n"
      "v_mfma_f32_16x16x32_bf16 v[52:55], a[84:87], v[164:167], v[52:55]\n"
      "ds_read_b64_tr_b16 a[152:153], v223 offset:51456\n"
      "v_mfma_f32_16x16x32_bf16 a[176:179], a[88:91], v[164:167], a[176:179]\n"
      "buffer_load_dwordx4 v[2:5], v255, s[48:51], s4 offen sc1\n"
      "v_mfma_f32_16x16x32_bf16 a[180:183], a[92:95], v[164:167], a[180:183]\n"
      "buffer_load_dwordx4 v[6:9], v255, s[48:51], s4 offen offset:1024 sc1\n"
      "s_waitcnt vmcnt(10)\n"
      "v_mfma_f32_16x16x32_bf16 v[48:51], a[96:99], v[168:171], v[48:51]\n"
      "v_max3_u32 v239, v176, v177, v178\n"
      "v_max3_u32 v239, v239, v179, v180\n"
      "v_mfma_f32_16x16x32_bf16 v[52:55], a[100:103], v[168:171], v[52:55]\n"
      "v_max3_u32 v239, v239, v181, v182\n"
      "v_max3_u32 v239, v239, v183, v184\n"
      "v_mfma_f32_16x16x32_bf16 a[176:179], a[104:107], v[168:171], a[176:179]\n"
      "buffer_load_dwordx4 v[10:13], v217, s[48:51], s4 offen offset:2048 sc1\n"
      "v_mfma_f32_16x16x32_bf16 a[180:183], a[108:111], v[168:171], a[180:183]\n"
      "s_add_u32 s4, s88, 0x21000\n"
      "v_max3_u32 v239, v239, v185, v186\n"
      "v_max3_u32 v239, v239, v187, v188\n"
      "v_mfma_f32_16x16x32_bf16 v[48:51], a[112:115], v[172:175], v[48:51]\n"
      "v_max3_u32 v239, v239, v189, v190\n"
      "v_max_u32 v239, v239, v191\n"
      "v_mfma_f32_16x16x32_bf16 v[52:55], a[116:119], v[172:175], v[52:55]\n"
      "buffer_load_dwordx4 v[18:21], v255, s[48:51], s4 offen sc1\n"
      "v_cmp_eq_u32 vcc, -1, v239\n"
      "s_nop 0\n"
      "s_cbranch_vccnz L23_%=\n"
      "L24_%=:\n"
      "v_mfma_f32_16x16x32_bf16 a[176:179], a[120:123], v[172:175], a[176:179]\n"
      "ds_read_b64_tr_b16 a[156:157], v223 offset:51464\n"
      "s_xor_b32 s70, s63, 1\n"
      "v_mfma_f32_16x16x32_bf16 a[180:183], a[124:127], v[172:175], a[180:183]\n"
      "ds_read_b64_tr_b16 a[146:147], v223 offset:53248\n"
      "s_mul_i32 s70, s70, 0x3000\n"
      "v_mfma_f32_16x16x32_bf16 a[192:195], a[64:67], v[176:179], a[192:195]\n"
      "buffer_load_dwordx4 v[22:25], v255, s[48:51], s4 offen offset:1024 sc1\n"
      "v_mfma_f32_16x16x32_bf16 a[196:199], a[68:71], v[176:179], a[196:199]\n"
      "ds_read_b64_tr_b16 a[150:151], v223 offset:53256\n"
      "s_add_u32 s70, s70, 0\n"
      "v_mfma_f32_16x16x32_bf16 a[208:211], a[72:75], v[176:179], a[208:211]\n"
      "ds_read_b64_tr_b16 a[154:155], v223 offset:53504\n"
      "v_add_u32 v236, s70, v255\n"
      "v_mfma_f32_16x16x32_bf16 a[212:215], a[76:79], v[176:179], a[212:215]\n"
      "s_waitcnt lgkmcnt(7)\n"
      "ds_read_b64_tr_b16 a[158:159], v223 offset:53512\n"
      "s_xor_b32 s70, s63, 2\n"
      "v_mfma_f32_16x16x32_bf16 a[192:195], a[80:83], v[180:183], a[192:195]\n"
      "buffer_load_dwordx4 v[26:29], v217, s[48:51], s4 offen offset:2048 sc1\n"
      "v_mfma_f32_16x16x32_bf16 a[196:199], a[84:87], v[180:183], a[196:199]\n"
      "s_add_u32 s4, s87, 0x20000\n"
      "ds_read_b64_tr_b16 a[160:161], v222\n"
      "s_mul_i32 s70, s70, 0x3000\n"
      "v_mfma_f32_16x16x32_bf16 a[208:211], a[88:91], v[180:183], a[208:211]\n"
      "s_waitcnt vmcnt(10)\n"
      "v_max3_u32 v239, v144, v145, v146\n"
      "v_max3_u32 v239, v239, v147, v148\n"
      "v_mfma_f32_16x16x32_bf16 a[212:215], a[92:95], v[180:183], a[212:215]\n"
      "buffer_load_dwordx4 v[96:99], v255, s[48:51], s4 offen sc1\n"
      "v_mfma_f32_16x16x32_bf16 a[192:195], a[96:99], v[184:187], a[192:195]\n"
      "v_max3_u32 v239, v239, v149, v150\n"
      "v_max3_u32 v239, v239, v151, v152\n"
      "v_mfma_f32_16x16x32_bf16 a[196:199], a[100:103], v[184:187], a[196:199]\n"
      "v_max3_u32 v239, v239, v153, v154\n"
      "v_max3_u32 v239, v239, v155, v156\n"
      "v_mfma_f32_16x16x32_bf16 a[208:211], a[104:107], v[184:187], a[208:211]\n"
      "buffer_load_dwordx4 v[100:103], v255, s[48:51], s4 offen offset:1024 sc1\n"
      "v_mfma_f32_16x16x32_bf16 a[212:215], a[108:111], v[184:187], a[212:215]\n"
      "v_max3_u32 v239, v239, v157, v158\n"
      "v_max_u32 v239, v239, v159\n"
      "v_mfma_f32_16x16x32_bf16 a[192:195], a[112:115], v[188:191], a[192:195]\n"
      "v_cmp_eq_u32 vcc, -1, v239\n"
      "s_nop 0\n"
      "s_cbranch_vccnz L25_%=\n"
      "L26_%=:\n"
      "v_mfma_f32_16x16x32_bf16 a[196:199], a[116:119], v[188:191], a[196:199]\n"
      "buffer_load_dwordx4 v[104:107], v217, s[48:51], s4 offen offset:2048 sc1\n"
      "v_mfma_f32_16x16x32_bf16 a[208:211], a[120:123], v[188:191], a[208:211]\n"
      "s_add_u32 s4, s87, 0x21000\n"
      "ds_read_b64_tr_b16 a[164:165], v222 offset:8\n"
      "s_add_u32 s70, s70, 0x1000\n"
      "v_mfma_f32_16x16x32_bf16 a[212:215], a[124:127], v[188:191], a[212:215]\n"
      "ds_read_b64_tr_b16 a[168:169], v222 offset:256\n"
      "v_add_u32 v237, s70, v255\n"
      "v_mfma_f32_16x16x32_bf16 a[200:203], a[64:67], v[144:147], a[200:203]\n"
      "buffer_load_dwordx4 v[112:115], v255, s[48:51], s4 offen sc1\n"
      "v_mfma_f32_16x16x32_bf16 a[204:207], a[68:71], v[144:147], a[204:207]\n"
      "ds_read_b64_tr_b16 a[172:173], v222 offset:264\n"
      "s_xor_b32 s70, s63, 3\n"
      "v_mfma_f32_16x16x32_bf16 a[216:219], a[72:75], v[144:147], a[216:219]\n"
      "ds_read_b64_tr_b16 a[162:163], v223\n"
      "s_mul_i32 s70, s70, 0x3000\n"
      "v_mfma_f32_16x16x32_bf16 a[220:223], a[76:79], v[144:147], a[220:223]\n"
      "ds_read_b64_tr_b16 a[166:167], v223 offset:8\n"
      "s_add_u32 s70, s70, 0x2000\n"
      "v_mfma_f32_16x16x32_bf16 a[200:203], a[80:83], v[148:151], a[200:203]\n"
      "buffer_load_dwordx4 v[116:119], v255, s[48:51], s4 offen offset:1024 sc1\n"
      "v_mfma_f32_16x16x32_bf16 a[204:207], a[84:87], v[148:151], a[204:207]\n"
      "ds_read_b64_tr_b16 a[170:171], v223 offset:256\n"
      "v_add_u32 v238, s70, v255\n"
      "v_mfma_f32_16x16x32_bf16 a[216:219], a[88:91], v[148:151], a[216:219]\n"
      "s_waitcnt lgkmcnt(7)\n"
      "ds_read_b64_tr_b16 a[174:175], v223 offset:264\n"
      "s_waitcnt vmcnt(8)\n"
      "v_mfma_f32_16x16x32_bf16 a[220:223], a[92:95], v[148:151], a[220:223]\n"
      "buffer_load_dwordx4 v[120:123], v217, s[48:51], s4 offen offset:2048 sc1\n"
      "v_mfma_f32_16x16x32_bf16 a[200:203], a[96:99], v[152:155], a[200:203]\n"
      "v_max3_u32 v239, v2, v3, v4\n"
      "v_max3_u32 v239, v239, v5, v6\n"
      "v_mfma_f32_16x16x32_bf16 a[204:207], a[100:103], v[152:155], a[204:207]\n"
      "v_max3_u32 v239, v239, v7, v8\n"
      "v_max3_u32 v239, v239, v9, v10\n"
      "v_mfma_f32_16x16x32_bf16 a[216:219], a[104:107], v[152:155], a[216:219]\n"
      "v_max3_u32 v239, v239, v11, v12\n"
      "v_max_u32 v239, v239, v13\n"
      "v_mfma_f32_16x16x32_bf16 a[220:223], a[108:111], v[152:155], a[220:223]\n"
      "v_cmp_eq_u32 vcc, -1, v239\n"
      "s_nop 0\n"
      "s_cbranch_vccnz L27_%=\n"
      "L28_%=:\n"
      "v_mfma_f32_16x16x32_bf16 a[200:203], a[112:115], v[156:159], a[200:203]\n"
      "s_mul_i32 s70, s63, 0x3000\n"
      "v_add_u32 v235, s70, v255\n"
      "s_add_u32 s71, s85, 0x1000\n"
      "v_mfma_f32_16x16x32_bf16 a[204:207], a[116:119], v[156:159], a[204:207]\n"
      "v_mfma_f32_16x16x32_bf16 a[216:219], a[120:123], v[156:159], a[216:219]\n"
      "v_mfma_f32_16x16x32_bf16 a[220:223], a[124:127], v[156:159], a[220:223]\n"
      "v_mfma_f32_16x16x32_bf16 a[192:195], a[128:131], v[2:5], a[192:195]\n"
      "v_mfma_f32_16x16x32_bf16 a[196:199], a[132:135], v[2:5], a[196:199]\n"
      "v_mfma_f32_16x16x32_bf16 a[208:211], a[136:139], v[2:5], a[208:211]\n"
      "s_waitcnt vmcnt(6)\n"
      "v_max3_u32 v239, v18, v19, v20\n"
      "v_max3_u32 v239, v239, v21, v22\n"
      "v_mfma_f32_16x16x32_bf16 a[212:215], a[140:143], v[2:5], a[212:215]\n"
      "v_max3_u32 v239, v239, v23, v24\n"
      "v_max3_u32 v239, v239, v25, v26\n"
      "v_mfma_f32_16x16x32_bf16 a[192:195], a[144:147], v[6:9], a[192:195]\n"
      "v_max3_u32 v239, v239, v27, v28\n"
      "v_max_u32 v239, v239, v29\n"
      "v_mfma_f32_16x16x32_bf16 a[196:199], a[148:151], v[6:9], a[196:199]\n"
      "v_cmp_eq_u32 vcc, -1, v239\n"
      "s_nop 0\n"
      "s_cbranch_vccnz L29_%=\n"
      "L30_%=:\n"
      "v_mfma_f32_16x16x32_bf16 a[208:211], a[152:155], v[6:9], a[208:211]\n"
      "v_mfma_f32_16x16x32_bf16 a[212:215], a[156:159], v[6:9], a[212:215]\n"
      "s_waitcnt lgkmcnt(3)\n"
      "v_mfma_f32_16x16x32_bf16 a[192:195], a[160:163], v[10:13], a[192:195]\n"
      "s_barrier\n"
      "s_waitcnt lgkmcnt(2)\n"
      "v_mfma_f32_16x16x32_bf16 a[196:199], a[164:167], v[10:13], a[196:199]\n"
      "s_waitcnt lgkmcnt(1)\n"
      "v_mfma_f32_16x16x32_bf16 a[208:211], a[168:171], v[10:13], a[208:211]\n"
      "s_waitcnt lgkmcnt(0)\n"
      "v_mfma_f32_16x16x32_bf16 a[212:215], a[172:175], v[10:13], a[212:215]\n"
      "v_mfma_f32_16x16x32_bf16 a[200:203], a[128:131], v[18:21], a[200:203]\n"
      "v_mfma_f32_16x16x32_bf16 a[204:207], a[132:135], v[18:21], a[204:207]\n"
      "v_mfma_f32_16x16x32_bf16 a[216:219], a[136:139], v[18:21], a[216:219]\n"
      "v_max3_u32 v239, v128, v129, v130\n"
      "v_max3_u32 v239, v239, v131, v132\n"
      "v_mfma_f32_16x16x32_bf16 a[220:223], a[140:143], v[18:21], a[220:223]\n"
      "v_max3_u32 v239, v239, v133, v134\n"
      "v_max3_u32 v239, v239, v135, v136\n"
      "v_mfma_f32_16x16x32_bf16 a[200:203], a[144:147], v[22:25], a[200:203]\n"
      "v_max3_u32 v239, v239, v137, v138\n"
      "v_max3_u32 v239, v239, v139, v140\n"
      "v_mfma_f32_16x16x32_bf16 a[204:207], a[148:151], v[22:25], a[204:207]\n"
      "v_max3_u32 v239, v239, v141, v142\n"
      "v_max_u32 v239, v239, v143\n"
      "v_mfma_f32_16x16x32_bf16 a[216:219], a[152:155], v[22:25], a[216:219]\n"
      "v_cmp_eq_u32 vcc, -1, v239\n"
      "s_nop 0\n"
      "s_cbranch_vccnz L31_%=\n"
      "L32_%=:\n"
      "v_mfma_f32_16x16x32_bf16 a[220:223], a[156:159], v[22:25], a[220:223]\n"
      "v_mfma_f32_16x16x32_bf16 a[200:203], a[160:163], v[26:29], a[200:203]\n"
      "v_mfma_f32_16x16x32_bf16 a[204:207], a[164:167], v[26:29], a[204:207]\n"
      "v_mfma_f32_16x16x32_bf16 a[216:219], a[168:171], v[26:29], a[216:219]\n"
      "v_mfma_f32_16x16x32_bf16 a[220:223], a[172:175], v[26:29], a[220:223]\n"
      "v_mfma_f32_16x16x32_bf16 v[56:59], a[64:67], v[128:131], v[56:59]\n"
      "s_waitcnt vmcnt(3)\n"
      "v_max3_u32 v239, v96, v97, v98\n"
      "v_max3_u32 v239, v239, v99, v100\n"
      "v_mfma_f32_16x16x32_bf16 v[60:63], a[68:71], v[128:131], v[60:63]\n"
      "v_max3_u32 v239, v239, v101, v102\n"
      "v_max3_u32 v239, v239, v103, v104\n"
      "v_mfma_f32_16x16x32_bf16 a[184:187], a[72:75], v[128:131], a[184:187]\n"
      "ds_write_b128 v237, a[192:195]\n"
      "v_max3_u32 v239, v239, v105, v106\n"
      "v_mfma_f32_16x16x32_bf16 a[188:191], a[76:79], v[128:131], a[188:191]\n"
      "v_max_u32 v239, v239, v107\n"
      "v_cmp_eq_u32 vcc, -1, v239\n"
      "s_nop 0\n"
      "s_cbranch_vccnz L33_%=\n"
      "L34_%=:\n"
      "v_mfma_f32_16x16x32_bf16 v[56:59], a[80:83], v[132:135], v[56:59]\n"
      "ds_write_b128 v237, a[196:199] offset:1024\n"
      "s_waitcnt vmcnt(0)\n"
      "v_mfma_f32_16x16x32_bf16 v[60:63], a[84:87], v[132:135], v[60:63]\n"
      "v_max3_u32 v239, v112, v113, v114\n"
      "v_max3_u32 v239, v239, v115, v116\n"
      "v_mfma_f32_16x16x32_bf16 a[184:187], a[88:91], v[132:135], a[184:187]\n"
      "v_max3_u32 v239, v239, v117, v118\n"
      "v_max3_u32 v239, v239, v119, v120\n"
      "v_mfma_f32_16x16x32_bf16 a[188:191], a[92:95], v[132:135], a[188:191]\n"
      "v_max3_u32 v239, v239, v121, v122\n"
      "v_max_u32 v239, v239, v123\n"
      "v_mfma_f32_16x16x32_bf16 v[56:59], a[96:99], v[136:139], v[56:59]\n"
      "ds_write_b128 v237, a[200:203] offset:2048\n"
      "v_cmp_eq_u32 vcc, -1, v239\n"
      "s_nop 0\n"
      "s_cbranch_vccnz L35_%=\n"
      "L36_%=:\n"
      "v_mfma_f32_16x16x32_bf16 v[60:63], a[100:103], v[136:139], v[60:63]\n"
      "v_mfma_f32_16x16x32_bf16 a[184:187], a[104:107], v[136:139], a[184:187]\n"
      "ds_write_b128 v237, a[204:207] offset:3072\n"
      "v_mfma_f32_16x16x32_bf16 a[188:191], a[108:111], v[136:139], a[188:191]\n"
      "v_mfma_f32_16x16x32_bf16 v[56:59], a[112:115], v[140:143], v[56:59]\n"
      "v_mfma_f32_16x16x32_bf16 v[60:63], a[116:119], v[140:143], v[60:63]\n"
      "v_mfma_f32_16x16x32_bf16 a[184:187], a[120:123], v[140:143], a[184:187]\n"
      "ds_write_b128 v238, a[208:211]\n"
      "v_mfma_f32_16x16x32_bf16 a[188:191], a[124:127], v[140:143], a[188:191]\n"
      "v_mfma_f32_16x16x32_bf16 a[176:179], a[136:139], v[96:99], a[176:179]\n"
      "ds_write_b128 v238, a[212:215] offset:1024\n"
      "v_mfma_f32_16x16x32_bf16 a[180:183], a[140:143], v[96:99], a[180:183]\n"
      "v_mfma_f32_16x16x32_bf16 a[176:179], a[152:155], v[100:103], a[176:179]\n"
      "v_mfma_f32_16x16x32_bf16 a[180:183], a[156:159], v[100:103], a[180:183]\n"
      "v_mfma_f32_16x16x32_bf16 a[176:179], a[168:171], v[104:107], a[176:179]\n"
      "ds_write_b128 v238, a[216:219] offset:2048\n"
      "v_mfma_f32_16x16x32_bf16 a[180:183], a[172:175], v[104:107], a[180:183]\n"
      "v_mfma_f32_16x16x32_bf16 a[184:187], a[136:139], v[112:115], a[184:187]\n"
      "ds_write_b128 v238, a[220:223] offset:3072\n"
      "v_mfma_f32_16x16x32_bf16 a[188:191], a[140:143], v[112:115], a[188:191]\n"
      "v_mfma_f32_16x16x32_bf16 a[184:187], a[152:155], v[116:119], a[184:187]\n"
      "v_mfma_f32_16x16x32_bf16 a[188:191], a[156:159], v[116:119], a[188:191]\n"
      "v_mfma_f32_16x16x32_bf16 a[184:187], a[168:171], v[120:123], a[184:187]\n"
      "v_mfma_f32_16x16x32_bf16 a[188:191], a[172:175], v[120:123], a[188:191]\n"
      "v_mfma_f32_16x16x32_bf16 v[48:51], a[128:131], v[96:99], v[48:51]\n"
      "v_mfma_f32_16x16x32_bf16 v[52:55], a[132:135], v[96:99], v[52:55]\n"
      "v_mfma_f32_16x16x32_bf16 v[48:51], a[144:147], v[100:103], v[48:51]\n"
      "ds_write_b128 v236, a[176:179]\n"
      "v_mfma_f32_16x16x32_bf16 v[52:55], a[148:151], v[100:103], v[52:55]\n"
      "v_mfma_f32_16x16x32_bf16 v[48:51], a[160:163], v[104:107], v[48:51]\n"
      "ds_write_b128 v236, a[180:183] offset:1024\n"
      "v_mfma_f32_16x16x32_bf16 v[52:55], a[164:167], v[104:107], v[52:55]\n"
      "v_mfma_f32_16x16x32_bf16 v[56:59], a[128:131], v[112:115], v[56:59]\n"
      "ds_write_b128 v236, a[184:187] offset:2048\n"
      "v_mfma_f32_16x16x32_bf16 v[60:63], a[132:135], v[112:115], v[60:63]\n"
      "v_mfma_f32_16x16x32_bf16 v[56:59], a[144:147], v[116:119], v[56:59]\n"
      "ds_write_b128 v236, a[188:191] offset:3072\n"
      "v_mfma_f32_16x16x32_bf16 v[60:63], a[148:151], v[116:119], v[60:63]\n"
      "v_mfma_f32_16x16x32_bf16 v[56:59], a[160:163], v[120:123], v[56:59]\n"
      "v_mfma_f32_16x16x32_bf16 v[60:63], a[164:167], v[120:123], v[60:63]\n"
      "s_waitcnt lgkmcnt(0)\n"
      "s_barrier\n"
      "ds_read_b128 v[160:163], v235\n"
      "ds_read_b128 v[176:179], v235 offset:4096\n"
      "ds_read_b128 v[144:147], v235 offset:8192\n"
      "ds_read_b128 v[164:167], v235 offset:1024\n"
      "ds_read_b128 v[180:183], v235 offset:5120\n"
      "ds_read_b128 v[148:151], v235 offset:9216\n"
      "ds_read_b128 v[168:171], v235 offset:2048\n"
      "ds_read_b128 v[184:187], v235 offset:6144\n"
      "ds_read_b128 v[152:155], v235 offset:10240\n"
      "ds_read_b128 v[172:175], v235 offset:3072\n"
      "ds_read_b128 v[188:191], v235 offset:7168\n"
      "ds_read_b128 v[156:159], v235 offset:11264\n"
      "s_waitcnt lgkmcnt(11)\n"
      "v_pk_add_f32 v[48:49], v[48:49], v[160:161]\n"
      "v_pk_add_f32 v[50:51], v[50:51], v[162:163]\n"
      "s_waitcnt lgkmcnt(8)\n"
      "v_pk_add_f32 v[52:53], v[52:53], v[164:165]\n"
      "v_pk_add_f32 v[54:55], v[54:55], v[166:167]\n"
      "v_pk_add_f32 v[48:49], v[48:49], v[176:177]\n"
      "v_pk_add_f32 v[50:51], v[50:51], v[178:179]\n"
      "s_waitcnt lgkmcnt(7)\n"
      "v_pk_add_f32 v[52:53], v[52:53], v[180:181]\n"
      "v_pk_add_f32 v[54:55], v[54:55], v[182:183]\n"
      "v_pk_add_f32 v[48:49], v[48:49], v[144:145]\n"
      "v_pk_add_f32 v[50:51], v[50:51], v[146:147]\n"
      "s_waitcnt lgkmcnt(6)\n"
      "v_pk_add_f32 v[52:53], v[52:53], v[148:149]\n"
      "v_pk_add_f32 v[54:55], v[54:55], v[150:151]\n"
      "v_cvt_pk_bf16_f32 v192, v48, v49\n"
      "v_cvt_pk_bf16_f32 v193, v50, v51\n"
      "v_cvt_pk_bf16_f32 v194, v52, v53\n"
      "v_cvt_pk_bf16_f32 v195, v54, v55\n"
      "buffer_store_dwordx4 v[192:195], v216, s[28:31], s85 offen\n"
      "s_waitcnt lgkmcnt(5)\n"
      "v_pk_add_f32 v[56:57], v[56:57], v[168:169]\n"
      "v_pk_add_f32 v[58:59], v[58:59], v[170:171]\n"
      "s_waitcnt lgkmcnt(2)\n"
      "v_pk_add_f32 v[60:61], v[60:61], v[172:173]\n"
      "v_pk_add_f32 v[62:63], v[62:63], v[174:175]\n"
      "v_pk_add_f32 v[56:57], v[56:57], v[184:185]\n"
      "v_pk_add_f32 v[58:59], v[58:59], v[186:187]\n"
      "s_waitcnt lgkmcnt(1)\n"
      "v_pk_add_f32 v[60:61], v[60:61], v[188:189]\n"
      "v_pk_add_f32 v[62:63], v[62:63], v[190:191]\n"
      "v_pk_add_f32 v[56:57], v[56:57], v[152:153]\n"
      "v_pk_add_f32 v[58:59], v[58:59], v[154:155]\n"
      "s_waitcnt lgkmcnt(0)\n"
      "v_pk_add_f32 v[60:61], v[60:61], v[156:157]\n"
      "v_pk_add_f32 v[62:63], v[62:63], v[158:159]\n"
      "v_cvt_pk_bf16_f32 v196, v56, v57\n"
      "v_cvt_pk_bf16_f32 v197, v58, v59\n"
      "v_cvt_pk_bf16_f32 v198, v60, v61\n"
      "v_cvt_pk_bf16_f32 v199, v62, v63\n"
      "buffer_store_dwordx4 v[196:199], v216, s[28:31], s71 offen\n"
      "s_endpgm\n"
      "; out-of-line: reload a hand-off that was read too early\n"
      "L1_%=:\n"
      "s_mov_b32 s72, 0\n"
      "buffer_load_dwordx2 v[242:243], v244, s[32:35], s72 offen sc1\n"
      "s_add_u32 s73, s73, 1\n"
      "s_waitcnt vmcnt(0)\n"
      "v_cmp_ne_u64 vcc, s[60:61], v[242:243]\n"
      "s_cmp_ge_u32 s73, 0x8000\n"
      "s_cbranch_scc1 L2_%=\n"
      "s_cbranch_vccnz L1_%=\n"
      "s_branch L2_%=\n"
      "L3_%=:\n"
      "s_mov_b32 s72, 0x9000\n"
      "buffer_load_dwordx2 v[6:7], v47, s[32:35], s72 offen sc1\n"
      "s_add_u32 s73, s73, 1\n"
      "s_waitcnt vmcnt(0)\n"
      "v_lshlrev_b32 v254, 1, v246\n"
      "v_xor_b32 v254, v254, v6\n"
      "v_xor_b32 v253, v247, v7\n"
      "v_and_or_b32 v254, v254, -2, v253\n"
      "v_cmp_ne_u32 vcc, 0, v254\n"
      "s_cmp_ge_u32 s73, 0x8000\n"
      "s_cbranch_scc1 L4_%=\n"
      "s_cbranch_vccnz L3_%=\n"
      "s_branch L4_%=\n"
      "L5_%=:\n"
      "s_mov_b32 s72, 0x108000\n"
      "buffer_load_dwordx2 v[242:243], v241, s[48:51], s72 offen sc1\n"
      "s_add_u32 s73, s73, 1\n"
      "s_waitcnt vmcnt(0)\n"
      "v_cmp_ne_u64 vcc, s[60:61], v[242:243]\n"
      "s_cmp_ge_u32 s73, 0x8000\n"
      "s_cbranch_scc1 L6_%=\n"
      "s_cbranch_vccnz L5_%=\n"
      "s_branch L6_%=\n"
      "L7_%=:\n"
      "s_nop 1\n"
      "buffer_load_dwordx4 v[136:139], v255, s[48:51], s53 offen sc1\n"
      "buffer_load_dwordx4 v[140:143], v255, s[48:51], s53 offen offset:1024 sc1\n"
      "buffer_load_dwordx4 v[144:147], v255, s[48:51], s53 offen offset:2048 sc1\n"
      "buffer_load_dwordx4 v[148:151], v255, s[48:51], s53 offen offset:3072 sc1\n"
      "s_add_u32 s73, s73, 1\n"
      "s_waitcnt vmcnt(0)\n"
      "v_max3_u32 v239, v136, v137, v138\n"
      "v_max3_u32 v239, v239, v139, v140\n"
      "v_max3_u32 v239, v239, v141, v142\n"
      "v_max3_u32 v239, v239, v143, v144\n"
      "v_max3_u32 v239, v239, v145, v146\n"
      "v_max3_u32 v239, v239, v147, v148\n"
      "v_max3_u32 v239, v239, v149, v150\n"
      "v_max_u32 v239, v239, v151\n"
      "v_cmp_eq_u32 vcc, -1, v239\n"
      "s_cmp_ge_u32 s73, 0x8000\n"
      "s_cbranch_scc1 L8_%=\n"
      "s_cbranch_vccnz L7_%=\n"
      "s_branch L8_%=\n"
      "L9_%=:\n"
      "s_nop 1\n"
      "buffer_load_dwordx4 v[152:155], v255, s[48:51], s54 offen sc1\n"
      "buffer_load_dwordx4 v[156:159], v255, s[48:51], s54 offen offset:1024 sc1\n"
      "s_add_u32 s73, s73, 1\n"
      "s_waitcnt vmcnt(0)\n"
      "v_max3_u32 v239, v152, v153, v154\n"
      "v_max3_u32 v239, v239, v155, v156\n"
      "v_max3_u32 v239, v239, v157, v158\n"
      "v_max_u32 v239, v239, v159\n"
      "v_cmp_eq_u32 vcc, -1, v239\n"
      "s_cmp_ge_u32 s73, 0x8000\n"
      "s_cbranch_scc1 L10_%=\n"
      "s_cbranch_vccnz L9_%=\n"
      "s_branch L10_%=\n"
      "L11_%=:\n"
      "s_mov_b32 s72, 0x108800\n"
      "buffer_load_dwordx2 v[242:243], v240, s[48:51], s72 offen sc1\n"
      "s_add_u32 s73, s73, 1\n"
      "s_waitcnt vmcnt(0)\n"
      "v_cmp_ne_u64 vcc, s[60:61], v[242:243]\n"
      "s_cmp_ge_u32 s73, 0x8000\n"
      "s_cbranch_scc1 L12_%=\n"
      "s_cbranch_vccnz L11_%=\n"
      "s_branch L12_%=\n"
      "L13_%=:\n"
      "s_nop 1\n"
      "s_add_u32 s5, s88, 0\n"
      "buffer_load_dwordx4 v[18:21], v255, s[48:51], s5 offen sc1\n"
      "buffer_load_dwordx4 v[22:25], v255, s[48:51], s5 offen offset:1024 sc1\n"
      "buffer_load_dwordx4 v[26:29], v255, s[48:51], s5 offen offset:2048 sc1\n"
      "buffer_load_dwordx4 v[30:33], v255, s[48:51], s5 offen offset:3072 sc1\n"
      "s_add_u32 s73, s73, 1\n"
      "s_waitcnt vmcnt(0)\n"
      "v_max3_u32 v239, v18, v19, v20\n"
      "v_max3_u32 v239, v239, v21, v22\n"
      "v_max3_u32 v239, v239, v23, v24\n"
      "v_max3_u32 v239, v239, v25, v26\n"
      "v_max3_u32 v239, v239, v27, v28\n"
      "v_max3_u32 v239, v239, v29, v30\n"
      "v_max3_u32 v239, v239, v31, v32\n"
      "v_max_u32 v239, v239, v33\n"
      "v_cmp_eq_u32 vcc, -1, v239\n"
      "s_cmp_ge_u32 s73, 0x8000\n"
      "s_cbranch_scc1 L14_%=\n"
      "s_cbranch_vccnz L13_%=\n"
      "s_branch L14_%=\n"
      "L15_%=:\n"
      "s_nop 1\n"
      "s_add_u32 s5, s87, 0\n"
      "buffer_load_dwordx4 v[2:5], v255, s[48:51], s5 offen sc1\n"
      "buffer_load_dwordx4 v[6:9], v255, s[48:51], s5 offen offset:1024 sc1\n"
      "buffer_load_dwordx4 v[10:13], v255, s[48:51], s5 offen offset:2048 sc1\n"
      "buffer_load_dwordx4 v[14:17], v255, s[48:51], s5 offen offset:3072 sc1\n"
      "s_add_u32 s73, s73, 1\n"
      "s_waitcnt vmcnt(0)\n"
      "v_max3_u32 v239, v2, v3, v4\n"
      "v_max3_u32 v239, v239, v5, v6\n"
      "v_max3_u32 v239, v239, v7, v8\n"
      "v_max3_u32 v239, v239, v9, v10\n"
      "v_max3_u32 v239, v239, v11, v12\n"
      "v_max3_u32 v239, v239, v13, v14\n"
      "v_max3_u32 v239, v239, v15, v16\n"
      "v_max_u32 v239, v239, v17\n"
      "v_cmp_eq_u32 vcc, -1, v239\n"
      "s_cmp_ge_u32 s73, 0x8000\n"
      "s_cbranch_scc1 L16_%=\n"
      "s_cbranch_vccnz L15_%=\n"
      "s_branch L16_%=\n"
      "L17_%=:\n"
      "s_nop 1\n"
      "s_add_u32 s5, s87, 0x1000\n"
      "buffer_load_dwordx4 v[96:99], v255, s[48:51], s5 offen sc1\n"
      "buffer_load_dwordx4 v[100:103], v255, s[48:51], s5 offen offset:1024 sc1\n"
      "buffer_load_dwordx4 v[104:107], v255, s[48:51], s5 offen offset:2048 sc1\n"
      "buffer_load_dwordx4 v[108:111], v255, s[48:51], s5 offen offset:3072 sc1\n"
      "s_add_u32 s73, s73, 1\n"
      "s_waitcnt vmcnt(0)\n"
      "v_max3_u32 v239, v96, v97, v98\n"
      "v_max3_u32 v239, v239, v99, v100\n"
      "v_max3_u32 v239, v239, v101, v102\n"
      "v_max3_u32 v239, v239, v103, v104\n"
      "v_max3_u32 v239, v239, v105, v106\n"
      "v_max3_u32 v239, v239, v107, v108\n"
      "v_max3_u32 v239, v239, v109, v110\n"
      "v_max_u32 v239, v239, v111\n"
      "v_cmp_eq_u32 vcc, -1, v239\n"
      "s_cmp_ge_u32 s73, 0x8000\n"
      "s_cbranch_scc1 L18_%=\n"
      "s_cbranch_vccnz L17_%=\n"
      "s_branch L18_%=\n"
      "L19_%=:\n"
      "s_nop 1\n"
      "s_add_u32 s5, s88, 0x1000\n"
      "buffer_load_dwordx4 v[112:115], v255, s[48:51], s5 offen sc1\n"
      "buffer_load_dwordx4 v[116:119], v255, s[48:51], s5 offen offset:1024 sc1\n"
      "buffer_load_dwordx4 v[120:123], v255, s[48:51], s5 offen offset:2048 sc1\n"
      "buffer_load_dwordx4 v[124:127], v255, s[48:51], s5 offen offset:3072 sc1\n"
      "s_add_u32 s73, s73, 1\n"
      "s_waitcnt vmcnt(0)\n"
      "v_max3_u32 v239, v112, v113, v114\n"
      "v_max3_u32 v239, v239, v115, v116\n"
      "v_max3_u32 v239, v239, v117, v118\n"
      "v_max3_u32 v239, v239, v119, v120\n"
      "v_max3_u32 v239, v239, v121, v122\n"
      "v_max3_u32 v239, v239, v123, v124\n"
      "v_max3_u32 v239, v239, v125, v126\n"
      "v_max_u32 v239, v239, v127\n"
      "v_cmp_eq_u32 vcc, -1, v239\n"
      "s_cmp_ge_u32 s73, 0x8000\n"
      "s_cbranch_scc1 L20_%=\n"
      "s_cbranch_vccnz L19_%=\n"
      "s_branch L20_%=\n"
      "L21_%=:\n"
      "s_nop 1\n"
      "s_add_u32 s5, s87, 0x10000\n"
      "buffer_load_dwordx4 v[160:163], v255, s[48:51], s5 offen sc1\n"
      "buffer_load_dwordx4 v[164:167], v255, s[48:51], s5 offen offset:1024 sc1\n"
      "buffer_load_dwordx4 v[168:171], v255, s[48:51], s5 offen offset:2048 sc1\n"
      "buffer_load_dwordx4 v[172:175], v255, s[48:51], s5 offen offset:3072 sc1\n"
      "s_add_u32 s73, s73, 1\n"
      "s_waitcnt vmcnt(0)\n"
      "v_max3_u32 v239, v160, v161, v162\n"
      "v_max3_u32 v239, v239, v163, v164\n"
      "v_max3_u32 v239, v239, v165, v166\n"
      "v_max3_u32 v239, v239, v167, v168\n"
      "v_max3_u32 v239, v239, v169, v170\n"
      "v_max3_u32 v239, v239, v171, v172\n"
      "v_max3_u32 v239, v239, v173, v174\n"
      "v_max_u32 v239, v239, v175\n"
      "v_cmp_eq_u32 vcc, -1, v239\n"
      "s_cmp_ge_u32 s73, 0x8000\n"
      "s_cbranch_scc1 L22_%=\n"
      "s_cbranch_vccnz L21_%=\n"
      "s_branch L22_%=\n"
      "L23_%=:\n"
      "s_nop 1\n"
      "s_add_u32 s5, s88, 0x10000\n"
      "buffer_load_dwordx4 v[176:179], v255, s[48:51], s5 offen sc1\n"
      "buffer_load_dwordx4 v[180:183], v255, s[48:51], s5 offen offset:1024 sc1\n"
      "buffer_load_dwordx4 v[184:187], v255, s[48:51], s5 offen offset:2048 sc1\n"
      "buffer_load_dwordx4 v[188:191], v255, s[48:51], s5 offen offset:3072 sc1\n"
      "s_add_u32 s73, s73, 1\n"
      "s_waitcnt vmcnt(0)\n"
      "v_max3_u32 v239, v176, v177, v178\n"
      "v_max3_u32 v239, v239, v179, v180\n"
      "v_max3_u32 v239, v239, v181, v182\n"
      "v_max3_u32 v239, v239, v183, v184\n"
      "v_max3_u32 v239, v239, v185, v186\n"
      "v_max3_u32 v239, v239, v187, v188\n"
      "v_max3_u32 v239, v239, v189, v190\n"
      "v_max_u32 v239, v239, v191\n"
      "v_cmp_eq_u32 vcc, -1, v239\n"
      "s_cmp_ge_u32 s73, 0x8000\n"
      "s_cbranch_scc1 L24_%=\n"
      "s_cbranch_vccnz L23_%=\n"
      "s_branch L24_%=\n"
      "L25_%=:\n"
      "s_nop 1\n"
      "s_add_u32 s5, s88, 0x11000\n"
      "buffer_load_dwordx4 v[144:147], v255, s[48:51], s5 offen sc1\n"
      "buffer_load_dwordx4 v[148:151], v255, s[48:51], s5 offen offset:1024 sc1\n"
      "buffer_load_dwordx4 v[152:155], v255, s[48:51], s5 offen offset:2048 sc1\n"
      "buffer_load_dwordx4 v[156:159], v255, s[48:51], s5 offen offset:3072 sc1\n"
      "s_add_u32 s73, s73, 1\n"
      "s_waitcnt vmcnt(0)\n"
      "v_max3_u32 v239, v144, v145, v146\n"
      "v_max3_u32 v239, v239, v147, v148\n"
      "v_max3_u32 v239, v239, v149, v150\n"
      "v_max3_u32 v239, v239, v151, v152\n"
      "v_max3_u32 v239, v239, v153, v154\n"
      "v_max3_u32 v239, v239, v155, v156\n"
      "v_max3_u32 v239, v239, v157, v158\n"
      "v_max_u32 v239, v239, v159\n"
      "v_cmp_eq_u32 vcc, -1, v239\n"
      "s_cmp_ge_u32 s73, 0x8000\n"
      "s_cbranch_scc1 L26_%=\n"
      "s_cbranch_vccnz L25_%=\n"
      "s_branch L26_%=\n"
      "L27_%=:\n"
      "s_nop 1\n"
      "s_add_u32 s5, s88, 0x20000\n"
      "buffer_load_dwordx4 v[2:5], v255, s[48:51], s5 offen sc1\n"
      "buffer_load_dwordx4 v[6:9], v255, s[48:51], s5 offen offset:1024 sc1\n"
      "buffer_load_dwordx4 v[10:13], v217, s[48:51], s5 offen offset:2048 sc1\n"
      "s_add_u32 s73, s73, 1\n"
      "s_waitcnt vmcnt(0)\n"
      "v_max3_u32 v239, v2, v3, v4\n"
      "v_max3_u32 v239, v239, v5, v6\n"
      "v_max3_u32 v239, v239, v7, v8\n"
      "v_max3_u32 v239, v239, v9, v10\n"
      "v_max3_u32 v239, v239, v11, v12\n"
      "v_max_u32 v239, v239, v13\n"
      "v_cmp_eq_u32 vcc, -1, v239\n"
      "s_cmp_ge_u32 s73, 0x8000\n"
      "s_cbranch_scc1 L28_%=\n"
      "s_cbranch_vccnz L27_%=\n"
      "s_branch L28_%=\n"
      "L29_%=:\n"
      "s_nop 1\n"
      "s_add_u32 s5, s88, 0x21000\n"
      "buffer_load_dwordx4 v[18:21], v255, s[48:51], s5 offen sc1\n"
      "buffer_load_dwordx4 v[22:25], v255, s[48:51], s5 offen offset:1024 sc1\n"
      "buffer_load_dwordx4 v[26:29], v217, s[48:51], s5 offen offset:2048 sc1\n"
      "s_add_u32 s73, s73, 1\n"
      "s_waitcnt vmcnt(0)\n"
      "v_max3_u32 v239, v18, v19, v20\n"
      "v_max3_u32 v239, v239, v21, v22\n"
      "v_max3_u32 v239, v239, v23, v24\n"
      "v_max3_u32 v239, v239, v25, v26\n"
      "v_max3_u32 v239, v239, v27, v28\n"
      "v_max_u32 v239, v239, v29\n"
      "v_cmp_eq_u32 vcc, -1, v239\n"
      "s_cmp_ge_u32 s73, 0x8000\n"
      "s_cbranch_scc1 L30_%=\n"
      "s_cbranch_vccnz L29_%=\n"
      "s_branch L30_%=\n"
      "L31_%=:\n"
      "s_nop 1\n"
      "s_add_u32 s5, s87, 0x11000\n"
      "buffer_load_dwordx4 v[128:131], v255, s[48:51], s5 offen sc1\n"
      "buffer_load_dwordx4 v[132:135], v255, s[48:51], s5 offen offset:1024 sc1\n"
      "buffer_load_dwordx4 v[136:139], v255, s[48:51], s5 offen offset:2048 sc1\n"
      "buffer_load_dwordx4 v[140:143], v255, s[48:51], s5 offen offset:3072 sc1\n"
      "s_add_u32 s73, s73, 1\n"
      "s_waitcnt vmcnt(0)\n"
      "v_max3_u32 v239, v128, v129, v130\n"
      "v_max3_u32 v239, v239, v131, v132\n"
      "v_max3_u32 v239, v239, v133, v134\n"
      "v_max3_u32 v239, v239, v135, v136\n"
      "v_max3_u32 v239, v239, v137, v138\n"
      "v_max3_u32 v239, v239, v139, v140\n"
      "v_max3_u32 v239, v239, v141, v142\n"
      "v_max_u32 v239, v239, v143\n"
      "v_cmp_eq_u32 vcc, -1, v239\n"
      "s_cmp_ge_u32 s73, 0x8000\n"
      "s_cbranch_scc1 L32_%=\n"
      "s_cbranch_vccnz L31_%=\n"
      "s_branch L32_%=\n"
      "L33_%=:\n"
      "s_nop 1\n"
      "s_add_u32 s5, s87, 0x20000\n"
      "buffer_load_dwordx4 v[96:99], v255, s[48:51], s5 offen sc1\n"
      "buffer_load_dwordx4 v[100:103], v255, s[48:51], s5 offen offset:1024 sc1\n"
      "buffer_load_dwordx4 v[104:107], v217, s[48:51], s5 offen offset:2048 sc1\n"
      "s_add_u32 s73, s73, 1\n"
      "s_waitcnt vmcnt(0)\n"
      "v_max3_u32 v239, v96, v97, v98\n"
      "v_max3_u32 v239, v239, v99, v100\n"
      "v_max3_u32 v239, v239, v101, v102\n"
      "v_max3_u32 v239, v239, v103, v104\n"
      "v_max3_u32 v239, v239, v105, v106\n"
      "v_max_u32 v239, v239, v107\n"
      "v_cmp_eq_u32 vcc, -1, v239\n"
      "s_cmp_ge_u32 s73, 0x8000\n"
      "s_cbranch_scc1 L34_%=\n"
      "s_cbranch_vccnz L33_%=\n"
      "s_branch L34_%=\n"
      "L35_%=:\n"
      "s_nop 1\n"
      "s_add_u32 s5, s87, 0x21000\n"
      "buffer_load_dwordx4 v[112:115], v255, s[48:51], s5 offen sc1\n"
      "buffer_load_dwordx4 v[116:119], v255, s[48:51], s5 offen offset:1024 sc1\n"
      "buffer_load_dwordx4 v[120:123], v217, s[48:51], s5 offen offset:2048 sc1\n"
      "s_add_u32 s73, s73, 1\n"
      "s_waitcnt vmcnt(0)\n"
      "v_max3_u32 v239, v112, v113, v114\n"
      "v_max3_u32 v239, v239, v115, v116\n"
      "v_max3_u32 v239, v239, v117, v118\n"
      "v_max3_u32 v239, v239, v119, v120\n"
      "v_max3_u32 v239, v239, v121, v122\n"
      "v_max_u32 v239, v239, v123\n"
      "v_cmp_eq_u32 vcc, -1, v239\n"
      "s_cmp_ge_u32 s73, 0x8000\n"
      "s_cbranch_scc1 L36_%=\n"
      "s_cbranch_vccnz L35_%=\n"
      "s_branch L36_%=\n"
      "Lend_%=:\n"
      : "+{v0}"(tid)
      : "s"(__builtin_amdgcn_kernarg_segment_ptr()), "s"(blockIdx.x), "s"((unsigned)(size_t)lds)
      : "memory", "vcc", "scc", "s4", "s5", "s6", "s7", "s8", "s9", "s10", "s11", "s12", "s13", "s14", "s15", "s16",
        "s17", "s18", "s19", "s20", "s21", "s22", "s23", "s24", "s25", "s26", "s27", "s28", "s29", "s30", "s31", "s33",
        "s34", "s35", "s36", "s37", "s38", "s39", "s40", "s41", "s42", "s43", "s44", "s45", "s46", "s47", "s48", "s49",
        "s50", "s51", "s52", "s53", "s54", "s55", "s56", "s57", "s58", "s59", "s60", "s61", "s62", "s63", "s64", "s65",
        "s66", "s67", "s68", "s69", "s70", "s71", "s72", "s73", "s74", "s75", "s76", "s77", "s78", "s79", "s80", "s81",
        "s82", "s83", "s84", "s85", "s86", "s87", "s88", "s89", "s90", "s91", "s92", "s93", "s94", "s95", "s96", "s97",
        "s98", "s99", "v1", "v2", "v3", "v4", "v5", "v6", "v7", "v8", "v9", "v10", "v11", "v12", "v13", "v14",
        "v15", "v16", "v17", "v18", "v19", "v20", "v21", "v22", "v23", "v24", "v25", "v26", "v27", "v28", "v29", "v30",
        "v31", "v32", "v33", "v34", "v35", "v36", "v37", "v38", "v39", "v40", "v41", "v42", "v43", "v44", "v45", "v46",
        "v47", "v48", "v49", "v50", "v51", "v52", "v53", "v54", "v55", "v56", "v57", "v58", "v59", "v60", "v61", "v62",
        "v63", "v64", "v65", "v66", "v67", "v68", "v69", "v70", "v71", "v72", "v73", "v74", "v75", "v76", "v77", "v78",
        "v79", "v80", "v81", "v82", "v83", "v84", "v85", "v86", "v87", "v88", "v89", "v90", "v91", "v92", "v93", "v94",
        "v95", "v96", "v97", "v98", "v99", "v100", "v101", "v102", "v103", "v104", "v105", "v106", "v107", "v108", "v109", "v110",
        "v111", "v112", "v113", "v114", "v115", "v116", "v117", "v118", "v119", "v120", "v121", "v122", "v123", "v124", "v125", "v126",
        "v127", "v128", "v129", "v130", "v131", "v132", "v133", "v134", "v135", "v136", "v137", "v138", "v139", "v140", "v141", "v142",
        "v143", "v144", "v145", "v146", "v147", "v148", "v149", "v150", "v151", "v152", "v153", "v154", "v155", "v156", "v157", "v158",
        "v159", "v160", "v161", "v162", "v163", "v164", "v165", "v166", "v167", "v168", "v169", "v170", "v171", "v172", "v173", "v174",
        "v175", "v176", "v177", "v178", "v179", "v180", "v181", "v182", "v183", "v184", "v185", "v186", "v187", "v188", "v189", "v190",
        "v191", "v192", "v193", "v194", "v195", "v196", "v197", "v198", "v199", "v200", "v201", "v202", "v203", "v204", "v205", "v206",
        "v207", "v208", "v209", "v210", "v211", "v212", "v213", "v214", "v215", "v216", "v217", "v218", "v219", "v220", "v221", "v222",
        "v223", "v224", "v225", "v226", "v227", "v228", "v229", "v230", "v231", "v232", "v233", "v234", "v235", "v236", "v237", "v238",
        "v239", "v240", "v241", "v242", "v243", "v244", "v245", "v246", "v247", "v248", "v249", "v250", "v251", "v252", "v253", "v254",
        "v255", "a0", "a1", "a2", "a3", "a4", "a5", "a6", "a7", "a8", "a9", "a10", "a11", "a12", "a13", "a14",
        "a15", "a16", "a17", "a18", "a19", "a20", "a21", "a22", "a23", "a24", "a25", "a26", "a27", "a28", "a29", "a30",
        "a31", "a32", "a33", "a34", "a35", "a36", "a37", "a38", "a39", "a40", "a41", "a42", "a43", "a44", "a45", "a46",
        "a47", "a48", "a49", "a50", "a51", "a52", "a53", "a54", "a55", "a56", "a57", "a58", "a59", "a60", "a61", "a62",
        "a63", "a64", "a65", "a66", "a67", "a68", "a69", "a70", "a71", "a72", "a73", "a74", "a75", "a76", "a77", "a78",
        "a79", "a80", "a81", "a82", "a83", "a84", "a85", "a86", "a87", "a88", "a89", "a90", "a91", "a92", "a93", "a94",
        "a95", "a96", "a97", "a98", "a99", "a100", "a101", "a102", "a103", "a104", "a105", "a106", "a107", "a108", "a109", "a110",
        "a111", "a112", "a113", "a114", "a115", "a116", "a117", "a118", "a119", "a120", "a121", "a122", "a123", "a124", "a125", "a126",
        "a127", "a128", "a129", "a130", "a131", "a132", "a133", "a134", "a135", "a136", "a137", "a138", "a139", "a140", "a141", "a142",
        "a143", "a144", "a145", "a146", "a147", "a148", "a149", "a150", "a151", "a152", "a153", "a154", "a155", "a156", "a157", "a158",
        "a159", "a160", "a161", "a162", "a163", "a164", "a165", "a166", "a167", "a168", "a169", "a170", "a171", "a172", "a173", "a174",
        "a175", "a176", "a177", "a178", "a179", "a180", "a181", "a182", "a183", "a184", "a185", "a186", "a187", "a188", "a189", "a190",
        "a191", "a192", "a193", "a194", "a195", "a196", "a197", "a198", "a199", "a200", "a201", "a202", "a203", "a204", "a205", "a206",
        "a207", "a208", "a209", "a210", "a211", "a212", "a213", "a214", "a215", "a216", "a217", "a218", "a219", "a220", "a221", "a222",
        "a223", "a224", "a225", "a226", "a227", "a228", "a229", "a230", "a231", "a232", "a233", "a234", "a235", "a236", "a237", "a238",
        "a239", "a240", "a241", "a242", "a243", "a244", "a245", "a246", "a247", "a248", "a249", "a250", "a251", "a252", "a253", "a254",
        "a255");
}

extern "C" void kernel_launch(void* const* d_in, const int* in_sizes, int n_in, void* d_out, int out_size,
                              void* d_ws, size_t ws_size, hipStream_t stream) {
  fnet_swiglu_kernel<<<256, 256, 0, stream>>>(d_in[0], d_in[1], d_in[2], d_in[3], d_in[4], d_in[6], d_in[7],
                                               d_in[8], d_out, d_ws);
}
```
